```python
import torch

def _softmax(x, axis=-1):
    return torch.softmax(x, dim=axis)
def _gelu(x):
    return torch.nn.functional.gelu(x, approximate="tanh")
def _silu(x):
    return torch.nn.functional.silu(x)
def _sigmoid(x):
    return torch.sigmoid(x)
def _layernorm(x, axis=-1, eps=1e-5):
    m = x.mean(dim=axis, keepdim=True)
    v = x.var(dim=axis, keepdim=True, unbiased=False)
    return (x - m) / torch.sqrt(v + eps)
def _scatter_add(tgt, idx, src, axis=0):
    return tgt.clone().index_add_(axis, idx, src)
def _segment_sum(src, seg, n):
    out = torch.zeros((n,) + src.shape[1:], dtype=src.dtype)
    return out.index_add_(0, seg, src)
def _pad(x, pw):
    flat = tuple(v for pair in reversed(pw) for v in pair)
    return torch.nn.functional.pad(x, flat)

kernel_name = 'sequential_preLN__sliding_block_cached__glu_gated_glu__decode'

def setup_inputs():

    _w = (lambda a: (lambda t: t.to(torch.bfloat16) if t.is_floating_point() else t)(torch.as_tensor(a)))
    def _A_setup():
        B, Sq, D = 2, 1, 512
        Hq, Hkv, Sk, Dh = 32, 8, 32768, 128
        assert Hq % Hkv == 0, (Hq, Hkv)
        return {
            "x": _w((lambda *s: torch.randn(*s, dtype=torch.bfloat16) * 0.05)(B, Sq, D)),
            "k_cache": _w((lambda *s: torch.randn(*s, dtype=torch.bfloat16) * 3.2)(B, Hkv, Sk, Dh)),
            "v_cache": _w((lambda *s: torch.randn(*s, dtype=torch.bfloat16) * 0.4)(B, Hkv, Sk, Dh)),
            "wq": _w((lambda *s: torch.randn(*s, dtype=torch.bfloat16) * 3.2)(D, Hq * Dh) * (D ** -0.5)),
            "wo": _w((lambda *s: torch.randn(*s, dtype=torch.bfloat16) * 0.4)(Hq * Dh, D) * ((Hq * Dh) ** -0.5)),
        }
    _A_in = _A_setup()

    def _B_setup():
        B, S, D, F = 2, 1, 512, 1792
        return {
            "x": _w((lambda *s: torch.randn(*s, dtype=torch.bfloat16) * 0.05)(B, S, D)),
            "w_gate": _w((lambda *s: torch.randn(*s, dtype=torch.bfloat16) * 0.1)(D, F)),
            "w_up": _w((lambda *s: torch.randn(*s, dtype=torch.bfloat16) * 0.05)(D, F)),
            "w_down": _w((lambda *s: torch.randn(*s, dtype=torch.bfloat16) * 0.05)(F, D)),
        }
    _B_in = _B_setup()

    return {
        "A_x": _A_in["x"],
        "A_k_cache": _A_in["k_cache"],
        "A_v_cache": _A_in["v_cache"],
        "A_wq": _A_in["wq"],
        "A_wo": _A_in["wo"],
        "B_x": _B_in["x"],
        "B_w_gate": _B_in["w_gate"],
        "B_w_up": _B_in["w_up"],
        "B_w_down": _B_in["w_down"],
    }

def reference(A_x, A_k_cache, A_v_cache, A_wq, A_wo, B_x, B_w_gate, B_w_up, B_w_down):
    def _A_ref(x, k_cache, v_cache, wq, wo):
        B, Sq, D = x.shape
        Hq, Dh = 32, 128
        Hkv, Sk = k_cache.shape[1], k_cache.shape[2]
        W = min(4096, max(256, Sk // 4))
        rep = Hq // Hkv
        q = (lambda x, *d: x.permute(*d))((x @ wq).reshape(B, Sq, Hq, Dh), 0, 2, 1, 3)
        k = (lambda x, n, axis: torch.repeat_interleave(x, n, dim=axis))(k_cache[:, :, Sk - W:, :], rep, axis=1)
        v = (lambda x, n, axis: torch.repeat_interleave(x, n, dim=axis))(v_cache[:, :, Sk - W:, :], rep, axis=1)
        scores = torch.einsum("bhqd,bhkd->bhqk", q, k) * (Dh ** -0.5)
        p = _softmax(scores, axis=-1)
        o = torch.einsum("bhqk,bhkd->bhqd", p, v)
        return (lambda x, *d: x.permute(*d))(o, 0, 2, 1, 3).reshape(B, Sq, Hq * Dh) @ wo

    def _B_ref(x, w_gate, w_up, w_down):
        g = torch.einsum("bsd,df->bsf", x, w_gate)
        u = torch.einsum("bsd,df->bsf", x, w_up)
        h = _sigmoid(g) * u
        return torch.einsum("bsf,fd->bsd", h, w_down)

    x = A_x
    h = x + _A_ref(_layernorm(x, axis=-1), A_k_cache, A_v_cache, A_wq, A_wo)
    h2 = h + _B_ref(_layernorm(h, axis=-1), B_w_gate, B_w_up, B_w_down)
    return h2
```

```cpp
#include <hip/hip_runtime.h>
#include <hip/hip_bf16.h>
#include <stdint.h>

typedef unsigned short u16;
typedef unsigned int u32;
typedef __attribute__((ext_vector_type(4))) unsigned u32x4;

constexpr int B = 2, D = 512, HQ = 32, HKV = 8, SK = 32768, DH = 128, W = 4096, F = 1792;
constexpr int NSPLIT = 16, KPB = W / NSPLIT;

__device__ __forceinline__ float bf2f(u16 v) { return __uint_as_float(((u32)v) << 16); }
__device__ __forceinline__ float bfr(float f) {
    u32 u = __float_as_uint(f);
    u = (u + 0x7fffu + ((u >> 16) & 1u)) & 0xffff0000u;
    return __uint_as_float(u);
}
__device__ __forceinline__ u16 f2bf(float f) { return (u16)(__float_as_uint(bfr(f)) >> 16); }

__device__ __forceinline__ float wave_sum(float v) {
    for (int o = 32; o > 0; o >>= 1) v += __shfl_xor(v, o);
    return v;
}
__device__ __forceinline__ float wave_max(float v) {
    for (int o = 32; o > 0; o >>= 1) v = fmaxf(v, __shfl_xor(v, o));
    return v;
}

__device__ void ln512(const float* xin, float* xout, float* red, int tid) {
    float a = xin[tid], b = xin[tid + 256];
    float s = wave_sum(a + b);
    if ((tid & 63) == 0) red[tid >> 6] = s;
    __syncthreads();
    float mean = (red[0] + red[1] + red[2] + red[3]) * (1.0f / 512.0f);
    __syncthreads();
    float da = a - mean, db = b - mean;
    float s2 = wave_sum(da * da + db * db);
    if ((tid & 63) == 0) red[tid >> 6] = s2;
    __syncthreads();
    float var = (red[0] + red[1] + red[2] + red[3]) * (1.0f / 512.0f);
    __syncthreads();
    float mb = bfr(mean), vb = bfr(var);
    float den = bfr(sqrtf(bfr(vb + 1e-5f)));
    xout[tid] = bfr(bfr(a - mb) / den);
    xout[tid + 256] = bfr(bfr(b - mb) / den);
    __syncthreads();
}

__global__ void __launch_bounds__(256) k_qproj(const u16* __restrict__ x, const u16* __restrict__ wq, float* __restrict__ q) {
    __shared__ float xs[2][512], ln[2][512], red[4], part[256][4];
    int tid = threadIdx.x;
    for (int i = tid; i < 1024; i += 256) xs[i >> 9][i & 511] = bf2f(x[i]);
    __syncthreads();
    ln512(xs[0], ln[0], red, tid);
    ln512(xs[1], ln[1], red, tid);
    int c0 = blockIdx.x * 16;
    int half = tid & 1, r0 = tid >> 1;
    float acc[2][8];
    for (int i = 0; i < 8; i++) acc[0][i] = acc[1][i] = 0.f;
    for (int j = 0; j < 4; j++) {
        int r = r0 + 128 * j;
        u32x4 w = *(const u32x4*)(wq + (size_t)r * 4096 + c0 + half * 8);
        float l0 = ln[0][r], l1 = ln[1][r];
        for (int i = 0; i < 4; i++) {
            float lo = __uint_as_float(w[i] << 16), hi = __uint_as_float(w[i] & 0xffff0000u);
            acc[0][2 * i] += l0 * lo; acc[0][2 * i + 1] += l0 * hi;
            acc[1][2 * i] += l1 * lo; acc[1][2 * i + 1] += l1 * hi;
        }
    }
    for (int bb = 0; bb < 2; bb++)
        for (int i = 0; i < 8; i++) {
            float v = acc[bb][i];
            for (int o = 32; o > 1; o >>= 1) v += __shfl_xor(v, o);
            acc[bb][i] = v;
        }
    __shared__ float wred[4][2][2][8];
    int lane = tid & 63, wv = tid >> 6;
    if (lane < 2) for (int bb = 0; bb < 2; bb++) for (int i = 0; i < 8; i++) wred[wv][lane][bb][i] = acc[bb][i];
    __syncthreads();
    if (tid < 32) {
        int bb = tid >> 4, c = tid & 15;
        float s = 0.f;
        for (int w2 = 0; w2 < 4; w2++) s += wred[w2][c >> 3][bb][c & 7];
        q[bb * 4096 + c0 + c] = bfr(s);
    }
}

__global__ void __launch_bounds__(256) k_attn(const float* __restrict__ q, const u16* __restrict__ kc, const u16* __restrict__ vc,
                                              float* __restrict__ pml, float* __restrict__ po) {
    __shared__ float qs[4][128];
    __shared__ float ps[4][256];
    __shared__ float red[4][4];
    __shared__ float ored[4][4][128];
    int tid = threadIdx.x, lane = tid & 63, wv = tid >> 6;
    int s = blockIdx.x & 15, kvh = (blockIdx.x >> 4) & 7, b = blockIdx.x >> 7;
    for (int i = tid; i < 512; i += 256) qs[i >> 7][i & 127] = q[b * 4096 + kvh * 512 + i];
    __syncthreads();
    const u16* kb = kc + ((size_t)(b * HKV + kvh) * SK + (SK - W) + s * KPB) * DH;
    const u16* vb = vc + ((size_t)(b * HKV + kvh) * SK + (SK - W) + s * KPB) * DH;
    float sc[4] = {0, 0, 0, 0};
    {
        const u16* kr = kb + (size_t)tid * DH;
        for (int j = 0; j < 16; j++) {
            u32x4 w = *(const u32x4*)(kr + j * 8);
            for (int i = 0; i < 4; i++) {
                float lo = __uint_as_float(w[i] << 16), hi = __uint_as_float(w[i] & 0xffff0000u);
                for (int h = 0; h < 4; h++) sc[h] += qs[h][j * 8 + 2 * i] * lo + qs[h][j * 8 + 2 * i + 1] * hi;
            }
        }
    }
    float m[4];
    for (int h = 0; h < 4; h++) {
        sc[h] = bfr(bfr(sc[h]) * 0.08838834764831845f);
        float mm = wave_max(sc[h]);
        if (lane == 0) red[wv][h] = mm;
    }
    __syncthreads();
    for (int h = 0; h < 4; h++) m[h] = fmaxf(fmaxf(red[0][h], red[1][h]), fmaxf(red[2][h], red[3][h]));
    __syncthreads();
    float l[4];
    for (int h = 0; h < 4; h++) {
        float p = __expf(sc[h] - m[h]);
        ps[h][tid] = p;
        float ss = wave_sum(p);
        if (lane == 0) red[wv][h] = ss;
    }
    __syncthreads();
    for (int h = 0; h < 4; h++) l[h] = red[0][h] + red[1][h] + red[2][h] + red[3][h];
    float acc[4][8];
    for (int h = 0; h < 4; h++) for (int i = 0; i < 8; i++) acc[h][i] = 0.f;
    int r = lane >> 4, c = lane & 15;
    for (int t = 0; t < 16; t++) {
        int key = wv * 64 + t * 4 + r;
        u32x4 w = *(const u32x4*)(vb + (size_t)key * DH + c * 8);
        for (int i = 0; i < 4; i++) {
            float lo = __uint_as_float(w[i] << 16), hi = __uint_as_float(w[i] & 0xffff0000u);
            for (int h = 0; h < 4; h++) { float p = ps[h][key]; acc[h][2 * i] += p * lo; acc[h][2 * i + 1] += p * hi; }
        }
    }
    for (int h = 0; h < 4; h++) for (int i = 0; i < 8; i++) {
        float v = acc[h][i];
        v += __shfl_xor(v, 16); v += __shfl_xor(v, 32);
        acc[h][i] = v;
    }
    if (r == 0) for (int h = 0; h < 4; h++) for (int i = 0; i < 8; i++) ored[wv][h][c * 8 + i] = acc[h][i];
    __syncthreads();
    for (int i = tid; i < 512; i += 256) {
        int h = i >> 7, d = i & 127;
        float v = ored[0][h][d] + ored[1][h][d] + ored[2][h][d] + ored[3][h][d];
        po[(size_t)blockIdx.x * 512 + i] = v;
    }
    if (tid < 4) { pml[blockIdx.x * 8 + tid * 2] = m[tid]; pml[blockIdx.x * 8 + tid * 2 + 1] = l[tid]; }
}

__global__ void __launch_bounds__(256) k_oproj(const float* __restrict__ pml, const float* __restrict__ po, const u16* __restrict__ wo,
                                               float* __restrict__ hpart) {
    __shared__ float os[32];
    __shared__ float ored[4][512];
    int tid = threadIdx.x, lane = tid & 63, wv = tid >> 6;
    int s = blockIdx.x & 15, kvh = (blockIdx.x >> 4) & 7, b = blockIdx.x >> 7;
    int grp = blockIdx.x & ~15;
    int h = s >> 2;
    if (tid < 32) {
        float M = -INFINITY;
        for (int j = 0; j < 16; j++) M = fmaxf(M, pml[(grp + j) * 8 + h * 2]);
        float L = 0.f, o = 0.f;
        for (int j = 0; j < 16; j++) {
            float w = __expf(pml[(grp + j) * 8 + h * 2] - M);
            L += w * pml[(grp + j) * 8 + h * 2 + 1];
            o += w * po[(size_t)(grp + j) * 512 + s * 32 + tid];
        }
        os[tid] = bfr(o / L);
    }
    __syncthreads();
    float acc[8];
    for (int i = 0; i < 8; i++) acc[i] = 0.f;
    for (int j = 0; j < 8; j++) {
        int rr = wv * 8 + j;
        u32x4 w = *(const u32x4*)(wo + (size_t)(kvh * 512 + s * 32 + rr) * 512 + lane * 8);
        float ov = os[rr];
        for (int i = 0; i < 4; i++) {
            acc[2 * i] += ov * __uint_as_float(w[i] << 16);
            acc[2 * i + 1] += ov * __uint_as_float(w[i] & 0xffff0000u);
        }
    }
    for (int i = 0; i < 8; i++) ored[wv][lane * 8 + i] = acc[i];
    __syncthreads();
    for (int i = tid; i < 512; i += 256) hpart[(size_t)blockIdx.x * 512 + i] = ored[0][i] + ored[1][i] + ored[2][i] + ored[3][i];
}

__global__ void __launch_bounds__(256) k_hred(const u16* __restrict__ x, const float* __restrict__ hpart, float* __restrict__ h) {
    int i = blockIdx.x * 256 + threadIdx.x;
    int b = i >> 9, c = i & 511;
    float s = 0.f;
    for (int j = 0; j < 128; j++) s += hpart[(size_t)(b * 128 + j) * 512 + c];
    h[i] = bfr(bf2f(x[i]) + bfr(s));
}

__global__ void __launch_bounds__(256) k_mlp(const float* __restrict__ h, const u16* __restrict__ wg, const u16* __restrict__ wu,
                                             const u16* __restrict__ wd, float* __restrict__ opart) {
    __shared__ float xs[2][512], ln[2][512], red[4];
    __shared__ float wred[4][4][2][2][8];
    __shared__ float hh[2][32];
    __shared__ float ored[4][2][512];
    int tid = threadIdx.x, lane = tid & 63, wv = tid >> 6;
    for (int i = tid; i < 1024; i += 256) xs[i >> 9][i & 511] = h[i];
    __syncthreads();
    ln512(xs[0], ln[0], red, tid);
    ln512(xs[1], ln[1], red, tid);
    int c0 = blockIdx.x * 32;
    int qd = tid & 3, r0 = tid >> 2;
    float ag[2][8], au[2][8];
    for (int i = 0; i < 8; i++) ag[0][i] = ag[1][i] = au[0][i] = au[1][i] = 0.f;
    for (int j = 0; j < 8; j++) {
        int r = r0 + 64 * j;
        u32x4 w = *(const u32x4*)(wg + (size_t)r * F + c0 + qd * 8);
        u32x4 w2 = *(const u32x4*)(wu + (size_t)r * F + c0 + qd * 8);
        float l0 = ln[0][r], l1 = ln[1][r];
        for (int i = 0; i < 4; i++) {
            float lo = __uint_as_float(w[i] << 16), hi = __uint_as_float(w[i] & 0xffff0000u);
            ag[0][2 * i] += l0 * lo; ag[0][2 * i + 1] += l0 * hi;
            ag[1][2 * i] += l1 * lo; ag[1][2 * i + 1] += l1 * hi;
            lo = __uint_as_float(w2[i] << 16); hi = __uint_as_float(w2[i] & 0xffff0000u);
            au[0][2 * i] += l0 * lo; au[0][2 * i + 1] += l0 * hi;
            au[1][2 * i] += l1 * lo; au[1][2 * i + 1] += l1 * hi;
        }
    }
    for (int bb = 0; bb < 2; bb++)
        for (int i = 0; i < 8; i++) {
            float v = ag[bb][i], u = au[bb][i];
            for (int o = 32; o > 2; o >>= 1) { v += __shfl_xor(v, o); u += __shfl_xor(u, o); }
            ag[bb][i] = v; au[bb][i] = u;
        }
    if (lane < 4) for (int bb = 0; bb < 2; bb++) for (int i = 0; i < 8; i++) { wred[wv][lane][0][bb][i] = ag[bb][i]; wred[wv][lane][1][bb][i] = au[bb][i]; }
    __syncthreads();
    if (tid < 64) {
        int bb = tid >> 5, c = tid & 31;
        float g = 0.f, u = 0.f;
        for (int w2 = 0; w2 < 4; w2++) { g += wred[w2][c >> 3][0][bb][c & 7]; u += wred[w2][c >> 3][1][bb][c & 7]; }
        g = bfr(g); u = bfr(u);
        float sg = bfr(1.0f / (1.0f + __expf(-g)));
        hh[bb][c] = bfr(sg * u);
    }
    __syncthreads();
    float acc[2][8];
    for (int i = 0; i < 8; i++) acc[0][i] = acc[1][i] = 0.f;
    for (int j = 0; j < 8; j++) {
        int rr = wv * 8 + j;
        u32x4 w = *(const u32x4*)(wd + (size_t)(c0 + rr) * 512 + lane * 8);
        float h0 = hh[0][rr], h1 = hh[1][rr];
        for (int i = 0; i < 4; i++) {
            float lo = __uint_as_float(w[i] << 16), hi = __uint_as_float(w[i] & 0xffff0000u);
            acc[0][2 * i] += h0 * lo; acc[0][2 * i + 1] += h0 * hi;
            acc[1][2 * i] += h1 * lo; acc[1][2 * i + 1] += h1 * hi;
        }
    }
    for (int i = 0; i < 8; i++) { ored[wv][0][lane * 8 + i] = acc[0][i]; ored[wv][1][lane * 8 + i] = acc[1][i]; }
    __syncthreads();
    for (int i = tid; i < 1024; i += 256) {
        int bb = i >> 9, c = i & 511;
        opart[(size_t)blockIdx.x * 1024 + i] = ored[0][bb][c] + ored[1][bb][c] + ored[2][bb][c] + ored[3][bb][c];
    }
}

__global__ void __launch_bounds__(256) k_final(const float* __restrict__ h, const float* __restrict__ opart, u16* __restrict__ out) {
    int i = blockIdx.x * 256 + threadIdx.x;
    float s = 0.f;
    for (int j = 0; j < 56; j++) s += opart[(size_t)j * 1024 + i];
    out[i] = f2bf(h[i] + bfr(s));
}

extern "C" void kernel_launch(void* const* d_in, const int* in_sizes, int n_in,
                              void* d_out, int out_size, void* d_ws, size_t ws_size,
                              hipStream_t stream) {
    const u16* x = (const u16*)d_in[0];
    const u16* kc = (const u16*)d_in[1];
    const u16* vc = (const u16*)d_in[2];
    const u16* wq = (const u16*)d_in[3];
    const u16* wo = (const u16*)d_in[4];
    const u16* wg = (const u16*)d_in[6];
    const u16* wu = (const u16*)d_in[7];
    const u16* wd = (const u16*)d_in[8];
    float* ws = (float*)d_ws;
    float* q = ws;
    float* pml = ws + 8192;
    float* po = ws + 16384;
    float* hpart = po + 256 * 512;
    float* h = hpart + 256 * 512;
    float* opart = h + 1024;
    k_qproj<<<256, 256, 0, stream>>>(x, wq, q);
    k_attn<<<256, 256, 0, stream>>>(q, kc, vc, pml, po);
    k_oproj<<<256, 256, 0, stream>>>(pml, po, wo, hpart);
    k_hred<<<4, 256, 0, stream>>>(x, hpart, h);
    k_mlp<<<56, 256, 0, stream>>>(h, wg, wu, wd, opart);
    k_final<<<4, 256, 0, stream>>>(h, opart, (u16*)d_out);
}
```

```cpp
#include <hip/hip_runtime.h>
#include <stdint.h>

typedef unsigned short u16;
typedef unsigned int u32;
typedef unsigned long long u64;
typedef __attribute__((ext_vector_type(4))) unsigned u32x4;
typedef __attribute__((ext_vector_type(2))) unsigned u32x2;
typedef __attribute__((ext_vector_type(8))) short s16x8;
typedef __attribute__((ext_vector_type(4))) float f32x4;
#define GAS __attribute__((address_space(1)))
typedef GAS u64 gu64;
typedef GAS u32 gu32;
#define RLX_AGENT __ATOMIC_RELAXED, __HIP_MEMORY_SCOPE_AGENT

constexpr int SK = 32768, WIN = 4096, FF = 1792;
constexpr int NBLK = 256, NTHR = 576, NMLP = 112;
constexpr unsigned SPIN_CAP = 1u << 18;

constexpr size_t G_EPOCH = 0;
constexpr size_t G_Q = 32;
constexpr size_t G_1O = G_Q + 4096;
constexpr size_t G_1ML = G_1O + 256 * 512;
constexpr size_t G_2 = G_1ML + 256 * 8;
constexpr size_t G_3 = G_2 + 256 * 512;
constexpr size_t G_4 = G_3 + 512;
constexpr size_t G_END = G_4 + (size_t)NMLP * 1024;

__device__ __forceinline__ float bf2f(u32 v) { return __uint_as_float(v << 16); }
__device__ __forceinline__ float bfr(float f) {
    u32 u = __float_as_uint(f);
    u = (u + 0x7fffu + ((u >> 16) & 1u)) & 0xffff0000u;
    return __uint_as_float(u);
}
__device__ __forceinline__ u32 packhi(float lo, float hi) {
    return __builtin_amdgcn_perm(__float_as_uint(hi), __float_as_uint(lo), 0x07060302u);
}
__device__ __forceinline__ u32x4 ldg(const u16* p) { return *(const u32x4*)p; }
__device__ __forceinline__ u32x4 ldg_nt(const u16* p) { return __builtin_nontemporal_load((const u32x4*)p); }
__device__ __forceinline__ f32x4 mfma(u32x4 a, u32x4 b, f32x4 c) {
    return __builtin_amdgcn_mfma_f32_16x16x32_bf16(__builtin_bit_cast(s16x8, a), __builtin_bit_cast(s16x8, b), c, 0, 0, 0);
}
__device__ __forceinline__ u32x4 pack2(f32x4 t0, f32x4 t1) {
    u32x4 r;
    r[0] = packhi(t0[0], t0[1]); r[1] = packhi(t0[2], t0[3]);
    r[2] = packhi(t1[0], t1[1]); r[3] = packhi(t1[2], t1[3]);
    return r;
}
__device__ __forceinline__ float wsum(float v) {
#pragma unroll
    for (int o = 32; o > 0; o >>= 1) v += __shfl_xor(v, o);
    return v;
}
__device__ __forceinline__ void barrier_raw() {
    asm volatile("s_waitcnt lgkmcnt(0)" ::: "memory");
    __builtin_amdgcn_s_barrier();
    asm volatile("" ::: "memory");
}
__device__ __forceinline__ void put(gu64* g, u32 tag, u32 val) {
    __hip_atomic_store(g, ((u64)tag << 32) | val, RLX_AGENT);
}
template <int N>
__device__ __forceinline__ void sweep(gu64* const (&p)[N], u32 tag, u32 (&v)[N]) {
    for (unsigned spins = 0; spins < SPIN_CAP; ++spins) {
        bool ok = true;
#pragma unroll
        for (int k = 0; k < N; ++k) {
            const u64 x = __hip_atomic_load(p[k], RLX_AGENT);
            v[k] = (u32)x;
            ok &= (u32)(x >> 32) == tag;
        }
        if (__all(ok)) return;
        __builtin_amdgcn_s_sleep(1);
    }
}

__device__ __forceinline__ void ln_rows(const u32x4 (&xr)[2], u32x4 (&out)[2]) {
#pragma unroll
    for (int b = 0; b < 2; ++b) {
        float v[8];
#pragma unroll
        for (int i = 0; i < 4; ++i) { v[2 * i] = bf2f(xr[b][i] & 0xffffu); v[2 * i + 1] = __uint_as_float(xr[b][i] & 0xffff0000u); }
        float s = 0.f;
#pragma unroll
        for (int i = 0; i < 8; ++i) s += v[i];
        const float mean = wsum(s) * (1.0f / 512.0f);
        float s2 = 0.f;
#pragma unroll
        for (int i = 0; i < 8; ++i) { const float d = v[i] - mean; s2 += d * d; }
        const float var = wsum(s2) * (1.0f / 512.0f);
        const float mb = bfr(mean);
        const float den = bfr(sqrtf(bfr(bfr(var) + 1e-5f)));
#pragma unroll
        for (int i = 0; i < 4; ++i)
            out[b][i] = packhi(bfr(bfr(v[2 * i] - mb) / den), bfr(bfr(v[2 * i + 1] - mb) / den));
    }
}

struct __attribute__((aligned(16))) Lds {
    float ow[8][512];
    float mw[8][4], lw[8][4];
    float qpart[8][32];
    float gupart[8][64];
    u16 lnw[8][2][64];
    u16 qs[512];
    u16 hs[1024];
    u16 os[32];
    u16 hh[2][16];
    u32 tag;
};

__global__ void __launch_bounds__(NTHR) mega(const u16* x, const u16* kc, const u16* vc, const u16* wq, const u16* wo,
                                             const u16* wg, const u16* wu, const u16* wd, u16* out, u64* wsg) {
    __shared__ Lds L;
    const int tid = threadIdx.x, lane = tid & 63;
    const int w = __builtin_amdgcn_readfirstlane(tid >> 6);
    const int bid = blockIdx.x;
    const int kvh = bid & 7, r = bid >> 3, b = r >> 4, s = r & 15;
    const bool is_mlp = r < 14;
    const int mblk = kvh * 14 + r;
    const int mc0 = mblk * 16;
    gu64* G = (gu64*)wsg;
    const int g = lane >> 4, li = lane & 15;

    if (w < 8) {
        u32x4 wqr[2], xr[2], kr[2][4], vr[2][4], wor[2][2], gur[4], wdr[2];
        const int qc0 = kvh * 512 + r * 16;
#pragma unroll
        for (int bb = 0; bb < 2; ++bb) xr[bb] = ldg(x + bb * 512 + lane * 8);
#pragma unroll
        for (int t = 0; t < 2; ++t)
            wqr[t] = ldg(wq + (size_t)(w * 64 + (g >> 1) * 32 + t * 16 + li) * 4096 + qc0 + (g & 1) * 8);
        asm volatile("" ::: "memory");
        const size_t kvoff = ((size_t)(b * 8 + kvh) * SK + (SK - WIN) + s * 256 + w * 32) * 128;
#pragma unroll
        for (int t = 0; t < 2; ++t)
#pragma unroll
            for (int kk = 0; kk < 4; ++kk) kr[t][kk] = ldg_nt(kc + kvoff + (size_t)(t * 16 + li) * 128 + kk * 32 + g * 8);
        asm volatile("" ::: "memory");
#pragma unroll
        for (int t = 0; t < 2; ++t)
#pragma unroll
            for (int kk = 0; kk < 4; ++kk) vr[t][kk] = ldg_nt(vc + kvoff + (size_t)(t * 16 + li) * 128 + kk * 32 + g * 8);
        asm volatile("" ::: "memory");
        __builtin_amdgcn_sched_barrier(0);

        u32x4 sel[2];
#pragma unroll
        for (int h = 0; h < 2; ++h) {
            const bool on = (g == 2 * h + (li >> 3));
            const u32 one = ((li & 1) ? 0x3F800000u : 0x00003F80u);
#pragma unroll
            for (int i = 0; i < 4; ++i) sel[h][i] = (on && ((li & 7) >> 1) == i) ? one : 0u;
        }
        const f32x4 zero4 = {0.f, 0.f, 0.f, 0.f};

        {
            u32x4 ln[2];
            ln_rows(xr, ln);
            if ((lane >> 3) == w) {
                *(u32x4*)&L.lnw[w][0][(lane & 7) * 8] = ln[0];
                *(u32x4*)&L.lnw[w][1][(lane & 7) * 8] = ln[1];
            }
            asm volatile("s_waitcnt lgkmcnt(0)" ::: "memory");
            f32x4 acc = zero4;
#pragma unroll
            for (int h = 0; h < 2; ++h) {
                const u32x2 a0 = *(const u32x2*)&L.lnw[w][li & 1][h * 32 + g * 4];
                const u32x2 a1 = *(const u32x2*)&L.lnw[w][li & 1][h * 32 + 16 + g * 4];
                const u32x4 bf = {a0[0], a0[1], a1[0], a1[1]};
                const f32x4 t0 = mfma(wqr[0], sel[h], zero4);
                const f32x4 t1 = mfma(wqr[1], sel[h], zero4);
                acc = mfma(pack2(t0, t1), bf, acc);
            }
            if (li < 2) *(f32x4*)&L.qpart[w][li * 16 + g * 4] = acc;
        }
        asm volatile("" ::: "memory");
#pragma unroll
        for (int t = 0; t < 2; ++t)
#pragma unroll
            for (int nc = 0; nc < 2; ++nc)
                wor[t][nc] = ldg(wo + (size_t)(kvh * 512 + s * 32 + t * 16 + li) * 512 + w * 64 + nc * 32 + g * 8);
        if (is_mlp) {
            const u16* gu = (g < 2) ? wg : wu;
#pragma unroll
            for (int t = 0; t < 4; ++t) gur[t] = ldg(gu + (size_t)(w * 64 + t * 16 + li) * FF + mc0 + (g & 1) * 8);
#pragma unroll
            for (int nc = 0; nc < 2; ++nc) wdr[nc] = ldg(wd + (size_t)(mc0 + li) * 512 + w * 64 + nc * 32 + g * 8);
        }
        barrier_raw();
        barrier_raw();

        {
            f32x4 st[2] = {zero4, zero4};
#pragma unroll
            for (int kk = 0; kk < 4; ++kk) {
                const u32x4 qf = *(const u32x4*)&L.qs[(li & 3) * 128 + kk * 32 + g * 8];
                st[0] = mfma(kr[0][kk], qf, st[0]);
                st[1] = mfma(kr[1][kk], qf, st[1]);
            }
            float sc[8];
#pragma unroll
            for (int t = 0; t < 2; ++t)
#pragma unroll
                for (int i = 0; i < 4; ++i) sc[t * 4 + i] = bfr(bfr(st[t][i]) * 0.08838834764831845f);
            float m = sc[0];
#pragma unroll
            for (int i = 1; i < 8; ++i) m = fmaxf(m, sc[i]);
            m = fmaxf(m, __shfl_xor(m, 16));
            m = fmaxf(m, __shfl_xor(m, 32));
            float p[8], l = 0.f;
#pragma unroll
            for (int i = 0; i < 8; ++i) { p[i] = __expf(sc[i] - m); l += p[i]; }
            l += __shfl_xor(l, 16);
            l += __shfl_xor(l, 32);
            u32x4 pf;
#pragma unroll
            for (int i = 0; i < 4; ++i) pf[i] = packhi(bfr(p[2 * i]), bfr(p[2 * i + 1]));
#pragma unroll
            for (int dt = 0; dt < 8; ++dt) {
                const f32x4 t0 = mfma(vr[0][dt >> 1], sel[dt & 1], zero4);
                const f32x4 t1 = mfma(vr[1][dt >> 1], sel[dt & 1], zero4);
                const f32x4 o = mfma(pack2(t0, t1), pf, zero4);
                if ((li >> 2) == (dt >> 1)) *(f32x4*)&L.ow[w][(li & 3) * 128 + dt * 16 + g * 4] = o;
            }
            if (lane < 4) { L.mw[w][lane] = m; L.lw[w][lane] = l; }
        }
        barrier_raw();
        const u32 tag = L.tag;
        {
            const int head = w >> 1, t = tid;
            float M = L.mw[0][head];
#pragma unroll
            for (int j = 1; j < 8; ++j) M = fmaxf(M, L.mw[j][head]);
            float o = 0.f, ls = 0.f;
#pragma unroll
            for (int j = 0; j < 8; ++j) {
                const float f = __expf(L.mw[j][head] - M);
                o += f * L.ow[j][t];
                ls += f * L.lw[j][head];
            }
            const int p1 = (kvh * 2 + b) * 16 + s;
            put(G + G_1O + (size_t)p1 * 512 + t, tag, __float_as_uint(o));
            if ((t & 127) == 0) {
                put(G + G_1ML + p1 * 8 + head * 2, tag, __float_as_uint(M));
                put(G + G_1ML + p1 * 8 + head * 2 + 1, tag, __float_as_uint(ls));
            }
        }
        barrier_raw();
        {
            const u32x2 a0 = *(const u32x2*)&L.os[g * 4];
            const u32x2 a1 = *(const u32x2*)&L.os[16 + g * 4];
            const u32x4 bf = {a0[0], a0[1], a1[0], a1[1]};
            float val = 0.f;
#pragma unroll
            for (int T = 0; T < 4; ++T) {
                const f32x4 t0 = mfma(wor[0][T >> 1], sel[T & 1], zero4);
                const f32x4 t1 = mfma(wor[1][T >> 1], sel[T & 1], zero4);
                const f32x4 d = mfma(pack2(t0, t1), bf, zero4);
                const float dv = (li & 2) ? ((li & 1) ? d[3] : d[2]) : ((li & 1) ? d[1] : d[0]);
                val = ((li >> 2) == T) ? dv : val;
            }
            put(G + G_2 + ((size_t)b * 128 + kvh * 16 + s) * 512 + w * 64 + (li >> 2) * 16 + g * 4 + (li & 3), tag, __float_as_uint(val));
        }
        if (is_mlp) {
            barrier_raw();
            u32x4 hr[2], ln[2];
            hr[0] = *(const u32x4*)&L.hs[lane * 8];
            hr[1] = *(const u32x4*)&L.hs[512 + lane * 8];
            ln_rows(hr, ln);
            if ((lane >> 3) == w) {
                *(u32x4*)&L.lnw[w][0][(lane & 7) * 8] = ln[0];
                *(u32x4*)&L.lnw[w][1][(lane & 7) * 8] = ln[1];
            }
            asm volatile("s_waitcnt lgkmcnt(0)" ::: "memory");
            f32x4 ag = zero4, au = zero4;
#pragma unroll
            for (int pp = 0; pp < 2; ++pp) {
                const u32x2 a0 = *(const u32x2*)&L.lnw[w][li & 1][pp * 32 + g * 4];
                const u32x2 a1 = *(const u32x2*)&L.lnw[w][li & 1][pp * 32 + 16 + g * 4];
                const u32x4 bf = {a0[0], a0[1], a1[0], a1[1]};
                const f32x4 g0 = mfma(gur[2 * pp], sel[0], zero4);
                const f32x4 g1 = mfma(gur[2 * pp + 1], sel[0], zero4);
                ag = mfma(pack2(g0, g1), bf, ag);
                const f32x4 u0 = mfma(gur[2 * pp], sel[1], zero4);
                const f32x4 u1 = mfma(gur[2 * pp + 1], sel[1], zero4);
                au = mfma(pack2(u0, u1), bf, au);
            }
            if (li < 2) {
                *(f32x4*)&L.gupart[w][li * 16 + g * 4] = ag;
                *(f32x4*)&L.gupart[w][32 + li * 16 + g * 4] = au;
            }
            barrier_raw();
            barrier_raw();
            {
                const u32x2 a0 = *(const u32x2*)&L.hh[li & 1][g * 4];
                const u32x4 bf = {a0[0], a0[1], 0u, 0u};
                float v0 = 0.f, v1 = 0.f;
                const int rep = li >> 1;
#pragma unroll
                for (int T = 0; T < 4; ++T) {
                    const f32x4 t0 = mfma(wdr[T >> 1], sel[T & 1], zero4);
                    const f32x4 d = mfma(pack2(t0, zero4), bf, zero4);
                    const bool mine = (rep >> 1) == T;
                    v0 = mine ? ((rep & 1) ? d[2] : d[0]) : v0;
                    v1 = mine ? ((rep & 1) ? d[3] : d[1]) : v1;
                }
                gu64* dst = G + G_4 + (size_t)mblk * 1024 + (li & 1) * 512 + w * 64 + (rep >> 1) * 16 + g * 4 + (rep & 1) * 2;
                put(dst, tag, __float_as_uint(v0));
                put(dst + 1, tag, __float_as_uint(v1));
            }
        }
    } else {
        const u32 ep = __hip_atomic_load((gu32*)(G + G_EPOCH), RLX_AGENT);
        const u32 tag = ep + 1u;
        const int rb = bid >> 7, rc = (bid & 127) * 4 + (lane & 3);
        const float xres = bf2f(x[rb * 512 + rc]);
        if (lane == 0) L.tag = tag;
        barrier_raw();
        {
            float q = 0.f;
            if (lane < 32) {
#pragma unroll
                for (int j = 0; j < 8; ++j) q += L.qpart[j][lane];
            }
            q = bfr(q);
            const float qn = __shfl_xor(q, 1);
            if (lane < 32 && (lane & 1) == 0) {
                const int bb = lane >> 4, n = lane & 15;
                put(G + G_Q + (size_t)kvh * 512 + bb * 256 + r * 8 + (n >> 1), tag, packhi(q, qn));
            }
        }
        {
            gu64* p[4]; u32 v[4];
#pragma unroll
            for (int k = 0; k < 4; ++k) p[k] = G + G_Q + (size_t)kvh * 512 + b * 256 + k * 64 + lane;
            sweep<4>(p, tag, v);
#pragma unroll
            for (int k = 0; k < 4; ++k) ((u32*)L.qs)[k * 64 + lane] = v[k];
        }
        barrier_raw();
        barrier_raw();
        {
            const int head = s >> 2, grp = (kvh * 2 + b) * 16;
            gu64* p[9]; u32 v[9];
#pragma unroll
            for (int i = 0; i < 8; ++i) p[i] = G + G_1O + (size_t)(grp + 2 * i + (lane >> 5)) * 512 + s * 32 + (lane & 31);
            p[8] = G + G_1ML + (size_t)(grp + ((lane & 31) >> 1)) * 8 + head * 2 + (lane & 1);
            sweep<9>(p, tag, v);
            const float ml = __uint_as_float(v[8]);
            float M = (lane & 1) ? -INFINITY : ml;
#pragma unroll
            for (int o = 16; o > 0; o >>= 1) M = fmaxf(M, __shfl_xor(M, o));
            const float mj = __shfl(ml, lane & 30);
            const float wj = __expf(mj - M);
            float ls = (lane & 1) ? wj * ml : 0.f;
#pragma unroll
            for (int o = 16; o > 0; o >>= 1) ls += __shfl_xor(ls, o);
            float o = 0.f;
#pragma unroll
            for (int i = 0; i < 8; ++i) o += __shfl(wj, 2 * (2 * i + (lane >> 5))) * __uint_as_float(v[i]);
            o += __shfl_xor(o, 32);
            const float ov = bfr(o / ls);
            if (lane < 32) L.os[lane] = (u16)(__float_as_uint(ov) >> 16);
        }
        barrier_raw();
        float hval;
        {
            gu64* p[8]; u32 v[8];
#pragma unroll
            for (int i = 0; i < 8; ++i) p[i] = G + G_2 + ((size_t)rb * 128 + i * 16 + (lane >> 2)) * 512 + rc;
            sweep<8>(p, tag, v);
            float sum = 0.f;
#pragma unroll
            for (int i = 0; i < 8; ++i) sum += __uint_as_float(v[i]);
#pragma unroll
            for (int o = 4; o < 64; o <<= 1) sum += __shfl_xor(sum, o);
            hval = bfr(xres + bfr(sum));
            const float hn = __shfl_xor(hval, 1);
            if (lane == 0 || lane == 2) put(G + G_3 + rb * 256 + (rc >> 1), tag, packhi(hval, hn));
        }
        if (is_mlp) {
            gu64* p[8]; u32 v[8];
#pragma unroll
            for (int i = 0; i < 8; ++i) p[i] = G + G_3 + i * 64 + lane;
            sweep<8>(p, tag, v);
#pragma unroll
            for (int i = 0; i < 8; ++i) ((u32*)L.hs)[i * 64 + lane] = v[i];
            barrier_raw();
            barrier_raw();
            {
                float a = 0.f;
#pragma unroll
                for (int j = 0; j < 8; ++j) a += L.gupart[j][lane];
                a = bfr(a);
                const float u = __shfl_xor(a, 32);
                const float sg = bfr(1.0f / (1.0f + __expf(-a)));
                const float hv = bfr(sg * u);
                if (lane < 32) L.hh[lane >> 4][lane & 15] = (u16)(__float_as_uint(hv) >> 16);
            }
            barrier_raw();
        }
        {
            gu64* p[7]; u32 v[7];
#pragma unroll
            for (int i = 0; i < 7; ++i) p[i] = G + G_4 + (size_t)(i * 16 + (lane >> 2)) * 1024 + rb * 512 + rc;
            sweep<7>(p, tag, v);
            float sum = 0.f;
#pragma unroll
            for (int i = 0; i < 7; ++i) sum += __uint_as_float(v[i]);
#pragma unroll
            for (int o = 4; o < 64; o <<= 1) sum += __shfl_xor(sum, o);
            const float res = bfr(hval + bfr(sum));
            if (lane < 4) out[rb * 512 + rc] = (u16)(__float_as_uint(res) >> 16);
        }
        if (bid == 0 && lane == 0) __hip_atomic_store((gu32*)(G + G_EPOCH), tag, RLX_AGENT);
    }
}

extern "C" void kernel_launch(void* const* d_in, const int* in_sizes, int n_in,
                              void* d_out, int out_size, void* d_ws, size_t ws_size,
                              hipStream_t stream) {
    mega<<<NBLK, NTHR, 0, stream>>>((const u16*)d_in[0], (const u16*)d_in[1], (const u16*)d_in[2], (const u16*)d_in[3],
                                    (const u16*)d_in[4], (const u16*)d_in[6], (const u16*)d_in[7], (const u16*)d_in[8],
                                    (u16*)d_out, (u64*)d_ws);
}
```

```cpp
#include <hip/hip_runtime.h>
#include <stdint.h>

typedef unsigned short u16;
typedef unsigned int u32;
typedef unsigned long long u64;
typedef __attribute__((ext_vector_type(4))) unsigned u32x4;
typedef __attribute__((ext_vector_type(2))) unsigned u32x2;
typedef __attribute__((ext_vector_type(8))) short s16x8;
typedef __attribute__((ext_vector_type(4))) float f32x4;
typedef __attribute__((ext_vector_type(2))) __bf16 bf16x2;
#define GAS __attribute__((address_space(1)))
typedef GAS u32 gu32;
#define RLX_AGENT __ATOMIC_RELAXED, __HIP_MEMORY_SCOPE_AGENT

constexpr int SK = 32768, WIN = 4096, FF = 1792;
constexpr int NBLK = 256, NTHR = 576, NMLP = 112;
constexpr unsigned SPIN_CAP = 1u << 18;

constexpr u32 O_EPOCH = 0;
constexpr u32 O_Q = 256;
constexpr u32 O_1O = O_Q + 8 * 2 * 128 * 16;
constexpr u32 O_1ML = O_1O + 256 * 256 * 16;
constexpr u32 O_2 = O_1ML + 256 * 4 * 16;
constexpr u32 O_3 = O_2 + 256 * 256 * 16;
constexpr u32 O_4 = O_3 + 256 * 16;
constexpr u32 O_END = O_4 + NMLP * 512 * 16;

#define STAMP(i) do { } while (0)
#define CSTAMP(i) do { } while (0)

__device__ __forceinline__ float lo2f(u32 v) { return __uint_as_float(v << 16); }
__device__ __forceinline__ float hi2f(u32 v) { return __uint_as_float(v & 0xffff0000u); }
__device__ __forceinline__ u32 pk(float lo, float hi) {
    const bf16x2 v = {(__bf16)lo, (__bf16)hi};
    return __builtin_bit_cast(u32, v);
}
__device__ __forceinline__ float bfr(float f) { return lo2f(pk(f, f)); }
__device__ __forceinline__ u32 packhi(float lo, float hi) {
    return __builtin_amdgcn_perm(__float_as_uint(hi), __float_as_uint(lo), 0x07060302u);
}
__device__ __forceinline__ u32x4 ldg(const u16* p) { return __builtin_nontemporal_load((const u32x4*)p); }
__device__ __forceinline__ f32x4 mfma(u32x4 a, u32x4 b, f32x4 c) {
    return __builtin_amdgcn_mfma_f32_16x16x32_bf16(__builtin_bit_cast(s16x8, a), __builtin_bit_cast(s16x8, b), c, 0, 0, 0);
}
__device__ __forceinline__ u32x4 pack2(f32x4 t0, f32x4 t1) {
    u32x4 r;
    r[0] = packhi(t0[0], t0[1]); r[1] = packhi(t0[2], t0[3]);
    r[2] = packhi(t1[0], t1[1]); r[3] = packhi(t1[2], t1[3]);
    return r;
}
template <int CTRL> __device__ __forceinline__ float dppf(float x) {
    return __builtin_bit_cast(float, __builtin_amdgcn_update_dpp(0, __builtin_bit_cast(int, x), CTRL, 0xf, 0xf, true));
}
template <int CTRL> __device__ __forceinline__ u32 dppu(u32 x) {
    return (u32)__builtin_amdgcn_update_dpp(0, (int)x, CTRL, 0xf, 0xf, true);
}
constexpr int XOR1 = 0xB1, XOR2 = 0x4E, HMIR = 0x141, MIR = 0x140, ROR4 = 0x124, ROR8 = 0x128;
__device__ __forceinline__ float xrow_sum(float x) {
    auto s = __builtin_amdgcn_permlane16_swap(__float_as_uint(x), __float_as_uint(x), false, false);
    x = __uint_as_float(s[0]) + __uint_as_float(s[1]);
    auto t = __builtin_amdgcn_permlane32_swap(__float_as_uint(x), __float_as_uint(x), false, false);
    return __uint_as_float(t[0]) + __uint_as_float(t[1]);
}
__device__ __forceinline__ float xrow_max(float x) {
    auto s = __builtin_amdgcn_permlane16_swap(__float_as_uint(x), __float_as_uint(x), false, false);
    x = fmaxf(__uint_as_float(s[0]), __uint_as_float(s[1]));
    auto t = __builtin_amdgcn_permlane32_swap(__float_as_uint(x), __float_as_uint(x), false, false);
    return fmaxf(__uint_as_float(t[0]), __uint_as_float(t[1]));
}
__device__ __forceinline__ float wsum(float v) {
    v += dppf<XOR1>(v); v += dppf<XOR2>(v); v += dppf<HMIR>(v); v += dppf<MIR>(v);
    return xrow_sum(v);
}
__device__ __forceinline__ void barrier_raw() {
    asm volatile("s_waitcnt lgkmcnt(0)" ::: "memory");
    __builtin_amdgcn_s_barrier();
    asm volatile("" ::: "memory");
}
typedef __amdgpu_buffer_rsrc_t rsrc_t;
__device__ __forceinline__ void put2(rsrc_t rs, u32 off, u32 tag, u32 v0, u32 v1) {
    const u32x4 d = {v0, tag, v1, tag};
    __builtin_amdgcn_raw_buffer_store_b128(d, rs, (int)off, 0, 16  );
}
template <int N>
__device__ __forceinline__ void sweep(rsrc_t rs, const u32 (&off)[N], u32 tag, u32x4 (&v)[N]) {
    for (unsigned spins = 0; spins < SPIN_CAP; ++spins) {
        asm volatile("" ::: "memory");
        bool ok = true;
#pragma unroll
        for (int k = 0; k < N; ++k) v[k] = __builtin_amdgcn_raw_buffer_load_b128(rs, (int)off[k], 0, 16  );
#pragma unroll
        for (int k = 0; k < N; ++k) ok &= (v[k][1] == tag) & (v[k][3] == tag);
        if (__all(ok)) return;
    }
}

__device__ __forceinline__ u32x4 ln_row(u32x4 xr) {
    float v[8];
#pragma unroll
    for (int i = 0; i < 4; ++i) { v[2 * i] = lo2f(xr[i]); v[2 * i + 1] = hi2f(xr[i]); }
    const float s = ((v[0] + v[1]) + (v[2] + v[3])) + ((v[4] + v[5]) + (v[6] + v[7]));
    const float mean = wsum(s) * (1.0f / 512.0f);
    float s2 = 0.f;
#pragma unroll
    for (int i = 0; i < 8; ++i) { const float d = v[i] - mean; s2 += d * d; }
    const float var = wsum(s2) * (1.0f / 512.0f);
    const float mb = bfr(mean);
    const float den = bfr(__builtin_amdgcn_sqrtf(bfr(bfr(var) + 1e-5f)));
    const float inv = __builtin_amdgcn_rcpf(den);
    u32x4 out;
#pragma unroll
    for (int i = 0; i < 4; ++i) {
        const u32 d = pk(v[2 * i] - mb, v[2 * i + 1] - mb);
        out[i] = pk(lo2f(d) * inv, hi2f(d) * inv);
    }
    return out;
}

struct __attribute__((aligned(16))) Lds {
    float ow[8][512];
    float mw[8][4], lw[8][4];
    float gupart[8][64];
    u16 ln[2][512];
    u16 qs[512];
    u16 os[32];
    u16 hh[2][16];
    u32 tag;
};

__global__ void __launch_bounds__(NTHR) mega(const u16* x, const u16* kc, const u16* vc, const u16* wq, const u16* wo,
                                             const u16* wg, const u16* wu, const u16* wd, u16* out, u32* wsg) {
    __shared__ Lds L;
    const int tid = threadIdx.x, lane = tid & 63;
    const int w = __builtin_amdgcn_readfirstlane(tid >> 6);
    const int bid = blockIdx.x;
    const int kvh = bid & 7, r = bid >> 3, b = r >> 4, s = r & 15;
    const bool is_mlp = r < 14;
    const int mblk = kvh * 14 + r;
    const int mc0 = mblk * 16;
    const rsrc_t G = __builtin_amdgcn_make_buffer_rsrc((void*)wsg, 0, (int)O_END, 0x00020000);
    const int g = lane >> 4, li = lane & 15;
    u32x4 sel[2];
#pragma unroll
    for (int h = 0; h < 2; ++h) {
        const bool on = (g == 2 * h + (li >> 3));
        const u32 one = ((li & 1) ? 0x3F800000u : 0x00003F80u);
#pragma unroll
        for (int i = 0; i < 4; ++i) sel[h][i] = (on && ((li & 7) >> 1) == i) ? one : 0u;
    }
    const f32x4 zero4 = {0.f, 0.f, 0.f, 0.f};

    if (w < 8) {
        u32x4 kr[2][4], vr[2][4], wor[2][2], gur[4], wdr[2];
        __builtin_amdgcn_s_barrier();
        asm volatile("" ::: "memory");
        const size_t kvoff = ((size_t)(b * 8 + kvh) * SK + (SK - WIN) + s * 256 + w * 32) * 128;
#pragma unroll
        for (int t = 0; t < 2; ++t)
#pragma unroll
            for (int kk = 0; kk < 4; ++kk) kr[t][kk] = ldg(kc + kvoff + (size_t)(t * 16 + li) * 128 + kk * 32 + g * 8);
        asm volatile("" ::: "memory");
#pragma unroll
        for (int t = 0; t < 2; ++t)
#pragma unroll
            for (int kk = 0; kk < 4; ++kk) vr[t][kk] = ldg(vc + kvoff + (size_t)(t * 16 + li) * 128 + kk * 32 + g * 8);
        asm volatile("" ::: "memory");
        CSTAMP(12);
        barrier_raw();
        CSTAMP(13);
#pragma unroll
        for (int t = 0; t < 2; ++t)
#pragma unroll
            for (int nc = 0; nc < 2; ++nc)
                wor[t][nc] = ldg(wo + (size_t)(kvh * 512 + s * 32 + t * 16 + li) * 512 + w * 64 + nc * 32 + g * 8);
        {
            const u16* gu = is_mlp ? ((g < 2) ? wg : wu) + (size_t)(w * 64 + li) * FF + mc0 + (g & 1) * 8 : wo;
            const size_t gstep = is_mlp ? (size_t)16 * FF : 0;
#pragma unroll
            for (int t = 0; t < 4; ++t) gur[t] = ldg(gu + t * gstep);
            const u16* dp = is_mlp ? wd + (size_t)(mc0 + li) * 512 + w * 64 + g * 8 : wo;
#pragma unroll
            for (int nc = 0; nc < 2; ++nc) wdr[nc] = ldg(dp + (is_mlp ? nc * 32 : 0));
        }
        asm volatile("" ::: "memory");

        {
            f32x4 st[2] = {zero4, zero4};
#pragma unroll
            for (int kk = 0; kk < 4; ++kk) {
                const u32x4 qf = *(const u32x4*)&L.qs[(li & 3) * 128 + kk * 32 + g * 8];
                st[0] = mfma(kr[0][kk], qf, st[0]);
                st[1] = mfma(kr[1][kk], qf, st[1]);
            }
            CSTAMP(14);
            float sc[8];
#pragma unroll
            for (int t = 0; t < 2; ++t)
#pragma unroll
                for (int i = 0; i < 2; ++i) {
                    const u32 a = pk(st[t][2 * i], st[t][2 * i + 1]);
                    const u32 c = pk(lo2f(a) * 0.08838834764831845f, hi2f(a) * 0.08838834764831845f);
                    sc[t * 4 + 2 * i] = lo2f(c); sc[t * 4 + 2 * i + 1] = hi2f(c);
                }
            float m = fmaxf(fmaxf(fmaxf(sc[0], sc[1]), fmaxf(sc[2], sc[3])), fmaxf(fmaxf(sc[4], sc[5]), fmaxf(sc[6], sc[7])));
            m = xrow_max(m);
            float p[8], l = 0.f;
#pragma unroll
            for (int i = 0; i < 8; ++i) { p[i] = __expf(sc[i] - m); l += p[i]; }
            l = xrow_sum(l);
            CSTAMP(15);
            u32x4 pf;
#pragma unroll
            for (int i = 0; i < 4; ++i) pf[i] = pk(p[2 * i], p[2 * i + 1]);
#pragma unroll
            for (int dt = 0; dt < 8; ++dt) {
                const f32x4 t0 = mfma(vr[0][dt >> 1], sel[dt & 1], zero4);
                const f32x4 t1 = mfma(vr[1][dt >> 1], sel[dt & 1], zero4);
                const f32x4 o = mfma(pack2(t0, t1), pf, zero4);
                *(f32x4*)&L.ow[w][(li & 3) * 128 + dt * 16 + g * 4] = o;
            }
            if (lane < 4) { L.mw[w][lane] = m; L.lw[w][lane] = l; }
        }
        CSTAMP(16);
        barrier_raw();
        CSTAMP(17);
        const u32 tag = L.tag;
        if (w < 4) {
            const int head = w;
            float M = L.mw[0][head];
#pragma unroll
            for (int j = 1; j < 8; ++j) M = fmaxf(M, L.mw[j][head]);
            float o0 = 0.f, o1 = 0.f, ls = 0.f;
#pragma unroll
            for (int j = 0; j < 8; ++j) {
                const float f = __expf(L.mw[j][head] - M);
                const float2 ov = *(const float2*)&L.ow[j][head * 128 + 2 * lane];
                o0 += f * ov.x; o1 += f * ov.y;
                ls += f * L.lw[j][head];
            }
            const u32 p1 = (kvh * 2 + b) * 16 + s;
            put2(G, O_1O + (p1 * 256 + head * 64 + lane) * 16, tag, __float_as_uint(o0), __float_as_uint(o1));
            if (lane == 0) put2(G, O_1ML + (p1 * 4 + head) * 16, tag, __float_as_uint(M), __float_as_uint(ls));
        }
        CSTAMP(18);
        u32x4 woT[4], guT[4], wdT[4];
#pragma unroll
        for (int T = 0; T < 4; ++T)
            woT[T] = pack2(mfma(wor[0][T >> 1], sel[T & 1], zero4), mfma(wor[1][T >> 1], sel[T & 1], zero4));
        if (is_mlp) {
#pragma unroll
            for (int pp = 0; pp < 2; ++pp)
#pragma unroll
                for (int mt = 0; mt < 2; ++mt)
                    guT[pp * 2 + mt] = pack2(mfma(gur[2 * pp], sel[mt], zero4), mfma(gur[2 * pp + 1], sel[mt], zero4));
#pragma unroll
            for (int T = 0; T < 4; ++T) wdT[T] = pack2(mfma(wdr[T >> 1], sel[T & 1], zero4), zero4);
        }
        barrier_raw();
        CSTAMP(19);
        {
            const u32x2 a0 = *(const u32x2*)&L.os[g * 4];
            const u32x2 a1 = *(const u32x2*)&L.os[16 + g * 4];
            const u32x4 bf = {a0[0], a0[1], a1[0], a1[1]};
            float v0 = 0.f, v1 = 0.f;
#pragma unroll
            for (int T = 0; T < 4; ++T) {
                const f32x4 d = mfma(woT[T], bf, zero4);
                const bool mine = (li >> 1) == T;
                v0 = mine ? ((li & 1) ? d[2] : d[0]) : v0;
                v1 = mine ? ((li & 1) ? d[3] : d[1]) : v1;
            }
            if (li < 8)
                put2(G, O_2 + ((b * 128 + kvh * 16 + s) * 256 + w * 32 + (li >> 1) * 8 + g * 2 + (li & 1)) * 16, tag,
                     __float_as_uint(v0), __float_as_uint(v1));
        }
        CSTAMP(20);
        if (is_mlp) {
            barrier_raw();
            CSTAMP(21);
            f32x4 ag = zero4, au = zero4;
#pragma unroll
            for (int pp = 0; pp < 2; ++pp) {
                const u32x2 a0 = *(const u32x2*)&L.ln[li & 1][w * 64 + pp * 32 + g * 4];
                const u32x2 a1 = *(const u32x2*)&L.ln[li & 1][w * 64 + pp * 32 + 16 + g * 4];
                const u32x4 bf = {a0[0], a0[1], a1[0], a1[1]};
                ag = mfma(guT[pp * 2], bf, ag);
                au = mfma(guT[pp * 2 + 1], bf, au);
            }
            if (li < 2) {
                *(f32x4*)&L.gupart[w][li * 16 + g * 4] = ag;
                *(f32x4*)&L.gupart[w][32 + li * 16 + g * 4] = au;
            }
            CSTAMP(23);
            barrier_raw();
            barrier_raw();
            CSTAMP(24);
            {
                const u32x2 a0 = *(const u32x2*)&L.hh[li & 1][g * 4];
                const u32x4 bf = {a0[0], a0[1], 0u, 0u};
                float v0 = 0.f, v1 = 0.f;
                const int rep = li >> 1;
#pragma unroll
                for (int T = 0; T < 4; ++T) {
                    const f32x4 d = mfma(wdT[T], bf, zero4);
                    const bool mine = (rep >> 1) == T;
                    v0 = mine ? ((rep & 1) ? d[2] : d[0]) : v0;
                    v1 = mine ? ((rep & 1) ? d[3] : d[1]) : v1;
                }
                put2(G, O_4 + (mblk * 512 + (li & 1) * 256 + w * 32 + (rep >> 1) * 8 + g * 2 + (rep & 1)) * 16, tag,
                     __float_as_uint(v0), __float_as_uint(v1));
            }
            CSTAMP(25);
        }
    } else {
        STAMP(0);
        const u32 ep = __hip_atomic_load((gu32*)(wsg), RLX_AGENT);
        const int rb = bid >> 7, rc = (bid & 127) * 4 + (lane & 1) * 2;
        const u32 xpair = *(const u32*)(x + rb * 512 + rc);
        u32x4 xr[2], wqr[16];
#pragma unroll
        for (int bb = 0; bb < 2; ++bb) xr[bb] = ldg(x + bb * 512 + lane * 8);
        {
            const u16* wp = wq + (size_t)((g >> 1) * 32 + li) * 4096 + kvh * 512 + r * 16 + (g & 1) * 8;
#pragma unroll
            for (int j = 0; j < 16; ++j) wqr[j] = ldg(wp + (size_t)((j >> 1) * 64 + (j & 1) * 16) * 4096);
        }
        asm volatile("" ::: "memory");
        __builtin_amdgcn_s_barrier();
        asm volatile("" ::: "memory");
        __builtin_amdgcn_sched_barrier(0);
        const u32 tag = ep + 1u;
        if (lane == 0) L.tag = tag;
        *(u32x4*)&L.ln[0][lane * 8] = ln_row(xr[0]);
        *(u32x4*)&L.ln[1][lane * 8] = ln_row(xr[1]);
        asm volatile("s_waitcnt lgkmcnt(0)" ::: "memory");
        STAMP(1);
        {
            f32x4 acc0 = zero4, acc1 = zero4;
#pragma unroll
            for (int jp = 0; jp < 8; ++jp) {
#pragma unroll
                for (int h = 0; h < 2; ++h) {
                    const u32x2 a0 = *(const u32x2*)&L.ln[li & 1][jp * 64 + h * 32 + g * 4];
                    const u32x2 a1 = *(const u32x2*)&L.ln[li & 1][jp * 64 + h * 32 + 16 + g * 4];
                    const u32x4 bf = {a0[0], a0[1], a1[0], a1[1]};
                    const u32x4 a2 = pack2(mfma(wqr[2 * jp], sel[h], zero4), mfma(wqr[2 * jp + 1], sel[h], zero4));
                    if (h == 0) acc0 = mfma(a2, bf, acc0); else acc1 = mfma(a2, bf, acc1);
                }
            }
            const u32 p01 = pk(acc0[0] + acc1[0], acc0[1] + acc1[1]), p23 = pk(acc0[2] + acc1[2], acc0[3] + acc1[3]);
            if (li < 2) put2(G, O_Q + ((kvh * 2 + li) * 128 + r * 4 + g) * 16, tag, p01, p23);
        }
        {
            u32 off[2]; u32x4 v[2];
#pragma unroll
            for (int k = 0; k < 2; ++k) off[k] = O_Q + ((kvh * 2 + b) * 128 + k * 64 + lane) * 16;
            sweep<2>(G, off, tag, v);
#pragma unroll
            for (int k = 0; k < 2; ++k) *(u32x2*)&L.qs[(k * 64 + lane) * 4] = u32x2{v[k][0], v[k][2]};
        }
        STAMP(2);
        barrier_raw();
        barrier_raw();
        STAMP(3);
        {
            const u32 head = s >> 2, grp = (kvh * 2 + b) * 16;
            u32 off[5]; u32x4 v[5];
#pragma unroll
            for (int i = 0; i < 4; ++i) off[i] = O_1O + ((grp + 4 * i + g) * 256 + s * 16 + li) * 16;
            off[4] = O_1ML + ((grp + li) * 4 + head) * 16;
            sweep<5>(G, off, tag, v);
            const float mj = __uint_as_float(v[4][0]), lj = __uint_as_float(v[4][2]);
            float M = fmaxf(mj, dppf<XOR1>(mj)); M = fmaxf(M, dppf<XOR2>(M)); M = fmaxf(M, dppf<HMIR>(M)); M = fmaxf(M, dppf<MIR>(M));
            const float wj = __expf(mj - M);
            float ls = wj * lj;
            ls += dppf<XOR1>(ls); ls += dppf<XOR2>(ls); ls += dppf<HMIR>(ls); ls += dppf<MIR>(ls);
            float o0 = 0.f, o1 = 0.f;
#pragma unroll
            for (int i = 0; i < 4; ++i) {
                const float w0 = __builtin_bit_cast(float, __builtin_amdgcn_readlane(__builtin_bit_cast(int, wj), 4 * i));
                const float w1 = __builtin_bit_cast(float, __builtin_amdgcn_readlane(__builtin_bit_cast(int, wj), 4 * i + 1));
                const float w2 = __builtin_bit_cast(float, __builtin_amdgcn_readlane(__builtin_bit_cast(int, wj), 4 * i + 2));
                const float w3 = __builtin_bit_cast(float, __builtin_amdgcn_readlane(__builtin_bit_cast(int, wj), 4 * i + 3));
                const float ww = (g & 2) ? ((g & 1) ? w3 : w2) : ((g & 1) ? w1 : w0);
                o0 += ww * __uint_as_float(v[i][0]);
                o1 += ww * __uint_as_float(v[i][2]);
            }
            o0 = xrow_sum(o0); o1 = xrow_sum(o1);
            const float inv = __builtin_amdgcn_rcpf(ls);
            if (lane < 16) ((u32*)L.os)[lane] = pk(o0 * inv, o1 * inv);
        }
        STAMP(4);
        barrier_raw();
        float h0, h1;
        {
            u32 off[4]; u32x4 v[4];
#pragma unroll
            for (int i = 0; i < 4; ++i) off[i] = O_2 + ((rb * 128 + i * 32 + (lane >> 1)) * 256 + (rc >> 1)) * 16;
            sweep<4>(G, off, tag, v);
            float s0 = 0.f, s1 = 0.f;
#pragma unroll
            for (int i = 0; i < 4; ++i) { s0 += __uint_as_float(v[i][0]); s1 += __uint_as_float(v[i][2]); }
            s0 += dppf<XOR2>(s0); s0 += dppf<ROR4>(s0); s0 += dppf<ROR8>(s0); s0 = xrow_sum(s0);
            s1 += dppf<XOR2>(s1); s1 += dppf<ROR4>(s1); s1 += dppf<ROR8>(s1); s1 = xrow_sum(s1);
            const u32 a = pk(s0, s1);
            const u32 pr = pk(lo2f(xpair) + lo2f(a), hi2f(xpair) + hi2f(a));
            h0 = lo2f(pr); h1 = hi2f(pr);
            const u32 pr2 = dppu<XOR1>(pr);
            if (lane == 0) put2(G, O_3 + bid * 16, tag, pr, pr2);
        }
        STAMP(5);
        if (is_mlp) {
            u32 off[4]; u32x4 v[4];
#pragma unroll
            for (int i = 0; i < 4; ++i) off[i] = O_3 + (i * 64 + lane) * 16;
            sweep<4>(G, off, tag, v);
            STAMP(6);
#pragma unroll
            for (int bb = 0; bb < 2; ++bb) {
                const u32x4 hr = {v[2 * bb][0], v[2 * bb][2], v[2 * bb + 1][0], v[2 * bb + 1][2]};
                const u32x4 ln = ln_row(hr);
                *(u32x2*)&L.ln[bb][lane * 4] = u32x2{ln[0], ln[1]};
                *(u32x2*)&L.ln[bb][256 + lane * 4] = u32x2{ln[2], ln[3]};
            }
            barrier_raw();
            barrier_raw();
            STAMP(7);
            {
                float a = 0.f;
#pragma unroll
                for (int j = 0; j < 8; ++j) a += L.gupart[j][lane];
                a = bfr(a);
                auto t = __builtin_amdgcn_permlane32_swap(__float_as_uint(a), __float_as_uint(a), false, false);
                const float u = __uint_as_float(t[1]);
                const float sg = bfr(__builtin_amdgcn_rcpf(1.0f + __expf(-a)));
                const float hv = bfr(sg * u);
                if (lane < 32) L.hh[lane >> 4][lane & 15] = (u16)(__float_as_uint(hv) >> 16);
            }
            barrier_raw();
        }
        STAMP(8);
        {
            u32 off[4]; u32x4 v[4];
#pragma unroll
            for (int i = 0; i < 4; ++i) {
                const u32 pidx = i * 32 + (lane >> 1);
                off[i] = O_4 + ((pidx < NMLP ? pidx : pidx - 16) * 512 + rb * 256 + (rc >> 1)) * 16;
            }
            sweep<4>(G, off, tag, v);
            float s0 = 0.f, s1 = 0.f;
#pragma unroll
            for (int i = 0; i < 4; ++i) {
                const bool live = (i * 32 + (lane >> 1)) < NMLP;
                s0 += live ? __uint_as_float(v[i][0]) : 0.f;
                s1 += live ? __uint_as_float(v[i][2]) : 0.f;
            }
            s0 += dppf<XOR2>(s0); s0 += dppf<ROR4>(s0); s0 += dppf<ROR8>(s0); s0 = xrow_sum(s0);
            s1 += dppf<XOR2>(s1); s1 += dppf<ROR4>(s1); s1 += dppf<ROR8>(s1); s1 = xrow_sum(s1);
            const u32 a = pk(s0, s1);
            const u32 res = pk(h0 + lo2f(a), h1 + hi2f(a));
            if (lane < 2) *(u32*)(out + rb * 512 + rc) = res;
        }
        STAMP(9);
        if (bid == 0 && lane == 0) __hip_atomic_store((gu32*)(wsg), tag, RLX_AGENT);
    }
}

extern "C" void kernel_launch(void* const* d_in, const int* in_sizes, int n_in,
                              void* d_out, int out_size, void* d_ws, size_t ws_size,
                              hipStream_t stream) {
    mega<<<NBLK, NTHR, 0, stream>>>((const u16*)d_in[0], (const u16*)d_in[1], (const u16*)d_in[2], (const u16*)d_in[3],
                                    (const u16*)d_in[4], (const u16*)d_in[6], (const u16*)d_in[7], (const u16*)d_in[8],
                                    (u16*)d_out, (u32*)d_ws);
}
```

```cpp
#include <hip/hip_runtime.h>
#include <stdint.h>

typedef unsigned short u16;
typedef unsigned int u32;
typedef unsigned long long u64;
typedef __attribute__((ext_vector_type(4))) unsigned u32x4;
typedef __attribute__((ext_vector_type(2))) unsigned u32x2;
typedef __attribute__((ext_vector_type(8))) short s16x8;
typedef __attribute__((ext_vector_type(4))) float f32x4;
typedef __attribute__((ext_vector_type(2))) __bf16 bf16x2;
#define GAS __attribute__((address_space(1)))
typedef GAS u32 gu32;
#define RLX_AGENT __ATOMIC_RELAXED, __HIP_MEMORY_SCOPE_AGENT

constexpr int SK = 32768, WIN = 4096, FF = 1792;
constexpr int NBLK = 256, NTHR = 576, NMLP = 112;
constexpr unsigned SPIN_CAP = 1u << 18;

constexpr u32 O_EPOCH = 0;
constexpr u32 O_Q = 256;
constexpr u32 O_1O = O_Q + 8 * 2 * 128 * 16;
constexpr u32 O_1ML = O_1O + 256 * 256 * 16;
constexpr u32 O_2 = O_1ML + 256 * 4 * 16;
constexpr u32 O_3 = O_2 + 256 * 256 * 16;
constexpr u32 O_4 = O_3 + 256 * 16;
constexpr u32 O_END = O_4 + NMLP * 512 * 16;

#define STAMP(i) do { } while (0)
#define CSTAMP(i) do { } while (0)

__device__ __forceinline__ float lo2f(u32 v) { return __uint_as_float(v << 16); }
__device__ __forceinline__ float hi2f(u32 v) { return __uint_as_float(v & 0xffff0000u); }
__device__ __forceinline__ u32 pk(float lo, float hi) {
    const bf16x2 v = {(__bf16)lo, (__bf16)hi};
    return __builtin_bit_cast(u32, v);
}
__device__ __forceinline__ float bfr(float f) { return lo2f(pk(f, f)); }
__device__ __forceinline__ u32 packhi(float lo, float hi) {
    return __builtin_amdgcn_perm(__float_as_uint(hi), __float_as_uint(lo), 0x07060302u);
}
__device__ __forceinline__ u32x4 ldg(const u16* p) { return __builtin_nontemporal_load((const u32x4*)p); }
__device__ __forceinline__ u32x4 ldc(const u16* p) { return *(const u32x4*)p; }
__device__ __forceinline__ f32x4 mfma(u32x4 a, u32x4 b, f32x4 c) {
    return __builtin_amdgcn_mfma_f32_16x16x32_bf16(__builtin_bit_cast(s16x8, a), __builtin_bit_cast(s16x8, b), c, 0, 0, 0);
}
__device__ __forceinline__ u32x4 pack2(f32x4 t0, f32x4 t1) {
    u32x4 r;
    r[0] = packhi(t0[0], t0[1]); r[1] = packhi(t0[2], t0[3]);
    r[2] = packhi(t1[0], t1[1]); r[3] = packhi(t1[2], t1[3]);
    return r;
}
template <int CTRL> __device__ __forceinline__ float dppf(float x) {
    return __builtin_bit_cast(float, __builtin_amdgcn_update_dpp(0, __builtin_bit_cast(int, x), CTRL, 0xf, 0xf, true));
}
template <int CTRL> __device__ __forceinline__ u32 dppu(u32 x) {
    return (u32)__builtin_amdgcn_update_dpp(0, (int)x, CTRL, 0xf, 0xf, true);
}
constexpr int XOR1 = 0xB1, XOR2 = 0x4E, HMIR = 0x141, MIR = 0x140, ROR4 = 0x124, ROR8 = 0x128;
__device__ __forceinline__ float xrow_sum(float x) {
    auto s = __builtin_amdgcn_permlane16_swap(__float_as_uint(x), __float_as_uint(x), false, false);
    x = __uint_as_float(s[0]) + __uint_as_float(s[1]);
    auto t = __builtin_amdgcn_permlane32_swap(__float_as_uint(x), __float_as_uint(x), false, false);
    return __uint_as_float(t[0]) + __uint_as_float(t[1]);
}
__device__ __forceinline__ float xrow_max(float x) {
    auto s = __builtin_amdgcn_permlane16_swap(__float_as_uint(x), __float_as_uint(x), false, false);
    x = fmaxf(__uint_as_float(s[0]), __uint_as_float(s[1]));
    auto t = __builtin_amdgcn_permlane32_swap(__float_as_uint(x), __float_as_uint(x), false, false);
    return fmaxf(__uint_as_float(t[0]), __uint_as_float(t[1]));
}
__device__ __forceinline__ float wsum(float v) {
    v += dppf<XOR1>(v); v += dppf<XOR2>(v); v += dppf<HMIR>(v); v += dppf<MIR>(v);
    return xrow_sum(v);
}
__device__ __forceinline__ void barrier_raw() {
    asm volatile("s_waitcnt lgkmcnt(0)" ::: "memory");
    __builtin_amdgcn_s_barrier();
    asm volatile("" ::: "memory");
}
typedef __amdgpu_buffer_rsrc_t rsrc_t;
__device__ __forceinline__ void put2(rsrc_t rs, u32 off, u32 tag, u32 v0, u32 v1) {
    const u32x4 d = {v0, tag, v1, tag};
    __builtin_amdgcn_raw_buffer_store_b128(d, rs, (int)off, 0, 16  );
}
template <int N>
__device__ __forceinline__ void sweep(rsrc_t rs, const u32 (&off)[N], u32 tag, u32x4 (&v)[N]) {
    for (unsigned spins = 0; spins < SPIN_CAP; ++spins) {
        asm volatile("" ::: "memory");
        bool ok = true;
#pragma unroll
        for (int k = 0; k < N; ++k) v[k] = __builtin_amdgcn_raw_buffer_load_b128(rs, (int)off[k], 0, 16  );
#pragma unroll
        for (int k = 0; k < N; ++k) ok &= (v[k][1] == tag) & (v[k][3] == tag);
        if (__all(ok)) return;
    }
}

__device__ __forceinline__ u32x4 ln_row(u32x4 xr) {
    float v[8];
#pragma unroll
    for (int i = 0; i < 4; ++i) { v[2 * i] = lo2f(xr[i]); v[2 * i + 1] = hi2f(xr[i]); }
    const float s = ((v[0] + v[1]) + (v[2] + v[3])) + ((v[4] + v[5]) + (v[6] + v[7]));
    const float mean = wsum(s) * (1.0f / 512.0f);
    float s2 = 0.f;
#pragma unroll
    for (int i = 0; i < 8; ++i) { const float d = v[i] - mean; s2 += d * d; }
    const float var = wsum(s2) * (1.0f / 512.0f);
    const float mb = bfr(mean);
    const float den = bfr(__builtin_amdgcn_sqrtf(bfr(bfr(var) + 1e-5f)));
    const float inv = __builtin_amdgcn_rcpf(den);
    u32x4 out;
#pragma unroll
    for (int i = 0; i < 4; ++i) {
        const u32 d = pk(v[2 * i] - mb, v[2 * i + 1] - mb);
        out[i] = pk(lo2f(d) * inv, hi2f(d) * inv);
    }
    return out;
}

struct __attribute__((aligned(16))) Lds {
    float ow[8][512];
    float mw[8][4], lw[8][4];
    float gupart[8][64];
    u16 ln[2][512];
    u16 qs[512];
    u16 os[32];
    u16 hh[2][16];
    u32 tag;
};

__global__ void __launch_bounds__(NTHR) mega(const u16* x, const u16* kc, const u16* vc, const u16* wq, const u16* wo,
                                             const u16* wg, const u16* wu, const u16* wd, u16* out, u32* wsg) {
    __shared__ Lds L;
    const int tid = threadIdx.x, lane = tid & 63;
    const int w = __builtin_amdgcn_readfirstlane(tid >> 6);
    const int bid = blockIdx.x;
    const int kvh = bid & 7, r = bid >> 3, b = r >> 4, s = r & 15;
    const bool is_mlp = r < 14;
    const int mblk = kvh * 14 + r;
    const int mc0 = mblk * 16;
    const rsrc_t G = __builtin_amdgcn_make_buffer_rsrc((void*)wsg, 0, (int)O_END, 0x00020000);
    const int g = lane >> 4, li = lane & 15;
    u32x4 sel[2];
#pragma unroll
    for (int h = 0; h < 2; ++h) {
        const bool on = (g == 2 * h + (li >> 3));
        const u32 one = ((li & 1) ? 0x3F800000u : 0x00003F80u);
#pragma unroll
        for (int i = 0; i < 4; ++i) sel[h][i] = (on && ((li & 7) >> 1) == i) ? one : 0u;
    }
    const f32x4 zero4 = {0.f, 0.f, 0.f, 0.f};

    if (w < 8) {
        u32x4 kr[2][4], vr[2][4], wor[2][2], gur[4], wdr[2];
        __builtin_amdgcn_s_barrier();
        asm volatile("" ::: "memory");
        const size_t kvoff = ((size_t)(b * 8 + kvh) * SK + (SK - WIN) + s * 256 + w * 32) * 128;
#pragma unroll
        for (int t = 0; t < 2; ++t)
#pragma unroll
            for (int kk = 0; kk < 4; ++kk) kr[t][kk] = ldg(kc + kvoff + (size_t)(t * 16 + li) * 128 + kk * 32 + g * 8);
        asm volatile("" ::: "memory");
#pragma unroll
        for (int t = 0; t < 2; ++t)
#pragma unroll
            for (int kk = 0; kk < 4; ++kk) vr[t][kk] = ldg(vc + kvoff + (size_t)(t * 16 + li) * 128 + kk * 32 + g * 8);
        asm volatile("" ::: "memory");
#pragma unroll
        for (int t = 0; t < 2; ++t)
#pragma unroll
            for (int nc = 0; nc < 2; ++nc)
                wor[t][nc] = ldc(wo + (size_t)(kvh * 512 + s * 32 + t * 16 + li) * 512 + w * 64 + nc * 32 + g * 8);
        if (is_mlp) {
            const u16* gu = (g < 2) ? wg : wu;
#pragma unroll
            for (int t = 0; t < 4; ++t) gur[t] = ldc(gu + (size_t)(w * 64 + t * 16 + li) * FF + mc0 + (g & 1) * 8);
#pragma unroll
            for (int nc = 0; nc < 2; ++nc) wdr[nc] = ldg(wd + (size_t)(mc0 + li) * 512 + w * 64 + nc * 32 + g * 8);
        }
        CSTAMP(12);
        barrier_raw();
        CSTAMP(13);

        {
            f32x4 st[2] = {zero4, zero4};
#pragma unroll
            for (int kk = 0; kk < 4; ++kk) {
                const u32x4 qf = *(const u32x4*)&L.qs[(li & 3) * 128 + kk * 32 + g * 8];
                st[0] = mfma(kr[0][kk], qf, st[0]);
                st[1] = mfma(kr[1][kk], qf, st[1]);
            }
            CSTAMP(14);
            float sc[8];
#pragma unroll
            for (int t = 0; t < 2; ++t)
#pragma unroll
                for (int i = 0; i < 2; ++i) {
                    const u32 a = pk(st[t][2 * i], st[t][2 * i + 1]);
                    const u32 c = pk(lo2f(a) * 0.08838834764831845f, hi2f(a) * 0.08838834764831845f);
                    sc[t * 4 + 2 * i] = lo2f(c); sc[t * 4 + 2 * i + 1] = hi2f(c);
                }
            float m = fmaxf(fmaxf(fmaxf(sc[0], sc[1]), fmaxf(sc[2], sc[3])), fmaxf(fmaxf(sc[4], sc[5]), fmaxf(sc[6], sc[7])));
            m = xrow_max(m);
            float p[8], l = 0.f;
#pragma unroll
            for (int i = 0; i < 8; ++i) { p[i] = __expf(sc[i] - m); l += p[i]; }
            l = xrow_sum(l);
            CSTAMP(15);
            u32x4 pf;
#pragma unroll
            for (int i = 0; i < 4; ++i) pf[i] = pk(p[2 * i], p[2 * i + 1]);
#pragma unroll
            for (int dt = 0; dt < 8; ++dt) {
                const f32x4 t0 = mfma(vr[0][dt >> 1], sel[dt & 1], zero4);
                const f32x4 t1 = mfma(vr[1][dt >> 1], sel[dt & 1], zero4);
                const f32x4 o = mfma(pack2(t0, t1), pf, zero4);
                *(f32x4*)&L.ow[w][(li & 3) * 128 + dt * 16 + g * 4] = o;
            }
            if (lane < 4) { L.mw[w][lane] = m; L.lw[w][lane] = l; }
        }
        CSTAMP(16);
        barrier_raw();
        CSTAMP(17);
        const u32 tag = L.tag;
        if (w < 4) {
            const int head = w;
            float M = L.mw[0][head];
#pragma unroll
            for (int j = 1; j < 8; ++j) M = fmaxf(M, L.mw[j][head]);
            float o0 = 0.f, o1 = 0.f, ls = 0.f;
#pragma unroll
            for (int j = 0; j < 8; ++j) {
                const float f = __expf(L.mw[j][head] - M);
                const float2 ov = *(const float2*)&L.ow[j][head * 128 + 2 * lane];
                o0 += f * ov.x; o1 += f * ov.y;
                ls += f * L.lw[j][head];
            }
            const u32 p1 = (kvh * 2 + b) * 16 + s;
            put2(G, O_1O + (p1 * 256 + head * 64 + lane) * 16, tag, __float_as_uint(o0), __float_as_uint(o1));
            if (lane == 0) put2(G, O_1ML + (p1 * 4 + head) * 16, tag, __float_as_uint(M), __float_as_uint(ls));
        }
        CSTAMP(18);
        u32x4 woT[4], guT[4], wdT[4];
#pragma unroll
        for (int T = 0; T < 4; ++T)
            woT[T] = pack2(mfma(wor[0][T >> 1], sel[T & 1], zero4), mfma(wor[1][T >> 1], sel[T & 1], zero4));
        if (is_mlp) {
#pragma unroll
            for (int pp = 0; pp < 2; ++pp)
#pragma unroll
                for (int mt = 0; mt < 2; ++mt)
                    guT[pp * 2 + mt] = pack2(mfma(gur[2 * pp], sel[mt], zero4), mfma(gur[2 * pp + 1], sel[mt], zero4));
#pragma unroll
            for (int T = 0; T < 4; ++T) wdT[T] = pack2(mfma(wdr[T >> 1], sel[T & 1], zero4), zero4);
        }
        barrier_raw();
        CSTAMP(19);
        {
            const u32x2 a0 = *(const u32x2*)&L.os[g * 4];
            const u32x2 a1 = *(const u32x2*)&L.os[16 + g * 4];
            const u32x4 bf = {a0[0], a0[1], a1[0], a1[1]};
            float v0 = 0.f, v1 = 0.f;
#pragma unroll
            for (int T = 0; T < 4; ++T) {
                const f32x4 d = mfma(woT[T], bf, zero4);
                const bool mine = (li >> 1) == T;
                v0 = mine ? ((li & 1) ? d[2] : d[0]) : v0;
                v1 = mine ? ((li & 1) ? d[3] : d[1]) : v1;
            }
            if (li < 8)
                put2(G, O_2 + ((b * 128 + kvh * 16 + s) * 256 + w * 32 + (li >> 1) * 8 + g * 2 + (li & 1)) * 16, tag,
                     __float_as_uint(v0), __float_as_uint(v1));
        }
        CSTAMP(20);
        if (is_mlp) {
            barrier_raw();
            CSTAMP(21);
            f32x4 ag = zero4, au = zero4;
#pragma unroll
            for (int pp = 0; pp < 2; ++pp) {
                const u32x2 a0 = *(const u32x2*)&L.ln[li & 1][w * 64 + pp * 32 + g * 4];
                const u32x2 a1 = *(const u32x2*)&L.ln[li & 1][w * 64 + pp * 32 + 16 + g * 4];
                const u32x4 bf = {a0[0], a0[1], a1[0], a1[1]};
                ag = mfma(guT[pp * 2], bf, ag);
                au = mfma(guT[pp * 2 + 1], bf, au);
            }
            if (li < 2) {
                *(f32x4*)&L.gupart[w][li * 16 + g * 4] = ag;
                *(f32x4*)&L.gupart[w][32 + li * 16 + g * 4] = au;
            }
            CSTAMP(23);
            barrier_raw();
            barrier_raw();
            CSTAMP(24);
            {
                const u32x2 a0 = *(const u32x2*)&L.hh[li & 1][g * 4];
                const u32x4 bf = {a0[0], a0[1], 0u, 0u};
                float v0 = 0.f, v1 = 0.f;
                const int rep = li >> 1;
#pragma unroll
                for (int T = 0; T < 4; ++T) {
                    const f32x4 d = mfma(wdT[T], bf, zero4);
                    const bool mine = (rep >> 1) == T;
                    v0 = mine ? ((rep & 1) ? d[2] : d[0]) : v0;
                    v1 = mine ? ((rep & 1) ? d[3] : d[1]) : v1;
                }
                put2(G, O_4 + (mblk * 512 + (li & 1) * 256 + w * 32 + (rep >> 1) * 8 + g * 2 + (rep & 1)) * 16, tag,
                     __float_as_uint(v0), __float_as_uint(v1));
            }
            CSTAMP(25);
        }
    } else {
        STAMP(0);
        const u32 ep = __hip_atomic_load((gu32*)(wsg), RLX_AGENT);
        const int rb = bid >> 7, rc = (bid & 127) * 4 + (lane & 1) * 2;
        const u32 xpair = *(const u32*)(x + rb * 512 + rc);
        u32x4 xr[2], wqr[16];
#pragma unroll
        for (int bb = 0; bb < 2; ++bb) xr[bb] = ldg(x + bb * 512 + lane * 8);
        {
            const u16* wp = wq + (size_t)((g >> 1) * 32 + li) * 4096 + kvh * 512 + r * 16 + (g & 1) * 8;
#pragma unroll
            for (int j = 0; j < 16; ++j) wqr[j] = ldc(wp + (size_t)((j >> 1) * 64 + (j & 1) * 16) * 4096);
        }
        asm volatile("" ::: "memory");
        __builtin_amdgcn_s_barrier();
        asm volatile("" ::: "memory");
        __builtin_amdgcn_sched_barrier(0);
        const u32 tag = ep + 1u;
        if (lane == 0) L.tag = tag;
        *(u32x4*)&L.ln[0][lane * 8] = ln_row(xr[0]);
        *(u32x4*)&L.ln[1][lane * 8] = ln_row(xr[1]);
        asm volatile("s_waitcnt lgkmcnt(0)" ::: "memory");
        STAMP(1);
        {
            f32x4 acc0 = zero4, acc1 = zero4;
#pragma unroll
            for (int jp = 0; jp < 8; ++jp) {
#pragma unroll
                for (int h = 0; h < 2; ++h) {
                    const u32x2 a0 = *(const u32x2*)&L.ln[li & 1][jp * 64 + h * 32 + g * 4];
                    const u32x2 a1 = *(const u32x2*)&L.ln[li & 1][jp * 64 + h * 32 + 16 + g * 4];
                    const u32x4 bf = {a0[0], a0[1], a1[0], a1[1]};
                    const u32x4 a2 = pack2(mfma(wqr[2 * jp], sel[h], zero4), mfma(wqr[2 * jp + 1], sel[h], zero4));
                    if (h == 0) acc0 = mfma(a2, bf, acc0); else acc1 = mfma(a2, bf, acc1);
                }
            }
            const u32 p01 = pk(acc0[0] + acc1[0], acc0[1] + acc1[1]), p23 = pk(acc0[2] + acc1[2], acc0[3] + acc1[3]);
            if (li < 2) put2(G, O_Q + ((kvh * 2 + li) * 128 + r * 4 + g) * 16, tag, p01, p23);
        }
        {
            u32 off[2]; u32x4 v[2];
#pragma unroll
            for (int k = 0; k < 2; ++k) off[k] = O_Q + ((kvh * 2 + b) * 128 + k * 64 + lane) * 16;
            sweep<2>(G, off, tag, v);
#pragma unroll
            for (int k = 0; k < 2; ++k) *(u32x2*)&L.qs[(k * 64 + lane) * 4] = u32x2{v[k][0], v[k][2]};
        }
        STAMP(2);
        barrier_raw();
        barrier_raw();
        STAMP(3);
        {
            const u32 head = s >> 2, grp = (kvh * 2 + b) * 16;
            u32 off[5]; u32x4 v[5];
#pragma unroll
            for (int i = 0; i < 4; ++i) off[i] = O_1O + ((grp + 4 * i + g) * 256 + s * 16 + li) * 16;
            off[4] = O_1ML + ((grp + li) * 4 + head) * 16;
            sweep<5>(G, off, tag, v);
            const float mj = __uint_as_float(v[4][0]), lj = __uint_as_float(v[4][2]);
            float M = fmaxf(mj, dppf<XOR1>(mj)); M = fmaxf(M, dppf<XOR2>(M)); M = fmaxf(M, dppf<HMIR>(M)); M = fmaxf(M, dppf<MIR>(M));
            const float wj = __expf(mj - M);
            float ls = wj * lj;
            ls += dppf<XOR1>(ls); ls += dppf<XOR2>(ls); ls += dppf<HMIR>(ls); ls += dppf<MIR>(ls);
            float o0 = 0.f, o1 = 0.f;
#pragma unroll
            for (int i = 0; i < 4; ++i) {
                const float w0 = __builtin_bit_cast(float, __builtin_amdgcn_readlane(__builtin_bit_cast(int, wj), 4 * i));
                const float w1 = __builtin_bit_cast(float, __builtin_amdgcn_readlane(__builtin_bit_cast(int, wj), 4 * i + 1));
                const float w2 = __builtin_bit_cast(float, __builtin_amdgcn_readlane(__builtin_bit_cast(int, wj), 4 * i + 2));
                const float w3 = __builtin_bit_cast(float, __builtin_amdgcn_readlane(__builtin_bit_cast(int, wj), 4 * i + 3));
                const float ww = (g & 2) ? ((g & 1) ? w3 : w2) : ((g & 1) ? w1 : w0);
                o0 += ww * __uint_as_float(v[i][0]);
                o1 += ww * __uint_as_float(v[i][2]);
            }
            o0 = xrow_sum(o0); o1 = xrow_sum(o1);
            const float inv = __builtin_amdgcn_rcpf(ls);
            if (lane < 16) ((u32*)L.os)[lane] = pk(o0 * inv, o1 * inv);
        }
        STAMP(4);
        barrier_raw();
        float h0, h1;
        {
            u32 off[4]; u32x4 v[4];
#pragma unroll
            for (int i = 0; i < 4; ++i) off[i] = O_2 + ((rb * 128 + i * 32 + (lane >> 1)) * 256 + (rc >> 1)) * 16;
            sweep<4>(G, off, tag, v);
            float s0 = 0.f, s1 = 0.f;
#pragma unroll
            for (int i = 0; i < 4; ++i) { s0 += __uint_as_float(v[i][0]); s1 += __uint_as_float(v[i][2]); }
            s0 += dppf<XOR2>(s0); s0 += dppf<ROR4>(s0); s0 += dppf<ROR8>(s0); s0 = xrow_sum(s0);
            s1 += dppf<XOR2>(s1); s1 += dppf<ROR4>(s1); s1 += dppf<ROR8>(s1); s1 = xrow_sum(s1);
            const u32 a = pk(s0, s1);
            const u32 pr = pk(lo2f(xpair) + lo2f(a), hi2f(xpair) + hi2f(a));
            h0 = lo2f(pr); h1 = hi2f(pr);
            const u32 pr2 = dppu<XOR1>(pr);
            if (lane == 0) put2(G, O_3 + bid * 16, tag, pr, pr2);
        }
        STAMP(5);
        if (is_mlp) {
            u32 off[4]; u32x4 v[4];
#pragma unroll
            for (int i = 0; i < 4; ++i) off[i] = O_3 + (i * 64 + lane) * 16;
            sweep<4>(G, off, tag, v);
            STAMP(6);
#pragma unroll
            for (int bb = 0; bb < 2; ++bb) {
                const u32x4 hr = {v[2 * bb][0], v[2 * bb][2], v[2 * bb + 1][0], v[2 * bb + 1][2]};
                const u32x4 ln = ln_row(hr);
                *(u32x2*)&L.ln[bb][lane * 4] = u32x2{ln[0], ln[1]};
                *(u32x2*)&L.ln[bb][256 + lane * 4] = u32x2{ln[2], ln[3]};
            }
            barrier_raw();
            barrier_raw();
            STAMP(7);
            {
                float a = 0.f;
#pragma unroll
                for (int j = 0; j < 8; ++j) a += L.gupart[j][lane];
                a = bfr(a);
                auto t = __builtin_amdgcn_permlane32_swap(__float_as_uint(a), __float_as_uint(a), false, false);
                const float u = __uint_as_float(t[1]);
                const float sg = bfr(__builtin_amdgcn_rcpf(1.0f + __expf(-a)));
                const float hv = bfr(sg * u);
                if (lane < 32) L.hh[lane >> 4][lane & 15] = (u16)(__float_as_uint(hv) >> 16);
            }
            barrier_raw();
        }
        STAMP(8);
        {
            u32 off[4]; u32x4 v[4];
#pragma unroll
            for (int i = 0; i < 4; ++i) {
                const u32 pidx = i * 32 + (lane >> 1);
                off[i] = O_4 + ((pidx < NMLP ? pidx : pidx - 16) * 512 + rb * 256 + (rc >> 1)) * 16;
            }
            sweep<4>(G, off, tag, v);
            float s0 = 0.f, s1 = 0.f;
#pragma unroll
            for (int i = 0; i < 4; ++i) {
                const bool live = (i * 32 + (lane >> 1)) < NMLP;
                s0 += live ? __uint_as_float(v[i][0]) : 0.f;
                s1 += live ? __uint_as_float(v[i][2]) : 0.f;
            }
            s0 += dppf<XOR2>(s0); s0 += dppf<ROR4>(s0); s0 += dppf<ROR8>(s0); s0 = xrow_sum(s0);
            s1 += dppf<XOR2>(s1); s1 += dppf<ROR4>(s1); s1 += dppf<ROR8>(s1); s1 = xrow_sum(s1);
            const u32 a = pk(s0, s1);
            const u32 res = pk(h0 + lo2f(a), h1 + hi2f(a));
            if (lane < 2) *(u32*)(out + rb * 512 + rc) = res;
        }
        STAMP(9);
        if (bid == 0 && lane == 0) __hip_atomic_store((gu32*)(wsg), tag, RLX_AGENT);
    }
}

extern "C" void kernel_launch(void* const* d_in, const int* in_sizes, int n_in,
                              void* d_out, int out_size, void* d_ws, size_t ws_size,
                              hipStream_t stream) {
    mega<<<NBLK, NTHR, 0, stream>>>((const u16*)d_in[0], (const u16*)d_in[1], (const u16*)d_in[2], (const u16*)d_in[3],
                                    (const u16*)d_in[4], (const u16*)d_in[6], (const u16*)d_in[7], (const u16*)d_in[8],
                                    (u16*)d_out, (u32*)d_ws);
}
```

```cpp
#include <hip/hip_runtime.h>
#include <stdint.h>
#include <stdio.h>

typedef unsigned short u16;
typedef unsigned int u32;
typedef unsigned long long u64;
typedef __attribute__((ext_vector_type(4))) unsigned u32x4;
typedef __attribute__((ext_vector_type(2))) unsigned u32x2;
typedef __attribute__((ext_vector_type(8))) short s16x8;
typedef __attribute__((ext_vector_type(4))) float f32x4;
typedef __attribute__((ext_vector_type(2))) __bf16 bf16x2;
#define GAS __attribute__((address_space(1)))
typedef GAS u32 gu32;
#define RLX_AGENT __ATOMIC_RELAXED, __HIP_MEMORY_SCOPE_AGENT

constexpr int SK = 32768, WIN = 4096, FF = 1792;
constexpr int NBLK = 256, NTHR = 576, NMLP = 112;
constexpr unsigned SPIN_CAP = 1u << 18;

constexpr u32 O_EPOCH = 0;
constexpr u32 O_Q = 256;
constexpr u32 O_1O = O_Q + 8 * 2 * 128 * 16;
constexpr u32 O_1ML = O_1O + 256 * 256 * 16;
constexpr u32 O_2 = O_1ML + 256 * 4 * 16;
constexpr u32 O_3 = O_2 + 256 * 256 * 16;
constexpr u32 O_4 = O_3 + 256 * 16;
constexpr u32 O_END = O_4 + NMLP * 512 * 16;

#define STAMP(i) do { tst[i] = __builtin_amdgcn_s_memrealtime(); } while (0)
#define CSTAMP(i) do { } while (0)

__device__ __forceinline__ float lo2f(u32 v) { return __uint_as_float(v << 16); }
__device__ __forceinline__ float hi2f(u32 v) { return __uint_as_float(v & 0xffff0000u); }
__device__ __forceinline__ u32 pk(float lo, float hi) {
    const bf16x2 v = {(__bf16)lo, (__bf16)hi};
    return __builtin_bit_cast(u32, v);
}
__device__ __forceinline__ float bfr(float f) { return lo2f(pk(f, f)); }
__device__ __forceinline__ u32 packhi(float lo, float hi) {
    return __builtin_amdgcn_perm(__float_as_uint(hi), __float_as_uint(lo), 0x07060302u);
}
__device__ __forceinline__ u32x4 ldg(const u16* p) { return __builtin_nontemporal_load((const u32x4*)p); }
__device__ __forceinline__ u32x4 ldc(const u16* p) { return *(const u32x4*)p; }
__device__ __forceinline__ f32x4 mfma(u32x4 a, u32x4 b, f32x4 c) {
    return __builtin_amdgcn_mfma_f32_16x16x32_bf16(__builtin_bit_cast(s16x8, a), __builtin_bit_cast(s16x8, b), c, 0, 0, 0);
}
__device__ __forceinline__ u32x4 pack2(f32x4 t0, f32x4 t1) {
    u32x4 r;
    r[0] = packhi(t0[0], t0[1]); r[1] = packhi(t0[2], t0[3]);
    r[2] = packhi(t1[0], t1[1]); r[3] = packhi(t1[2], t1[3]);
    return r;
}
template <int CTRL> __device__ __forceinline__ float dppf(float x) {
    return __builtin_bit_cast(float, __builtin_amdgcn_update_dpp(0, __builtin_bit_cast(int, x), CTRL, 0xf, 0xf, true));
}
template <int CTRL> __device__ __forceinline__ u32 dppu(u32 x) {
    return (u32)__builtin_amdgcn_update_dpp(0, (int)x, CTRL, 0xf, 0xf, true);
}
constexpr int XOR1 = 0xB1, XOR2 = 0x4E, HMIR = 0x141, MIR = 0x140, ROR4 = 0x124, ROR8 = 0x128;
__device__ __forceinline__ float xrow_sum(float x) {
    auto s = __builtin_amdgcn_permlane16_swap(__float_as_uint(x), __float_as_uint(x), false, false);
    x = __uint_as_float(s[0]) + __uint_as_float(s[1]);
    auto t = __builtin_amdgcn_permlane32_swap(__float_as_uint(x), __float_as_uint(x), false, false);
    return __uint_as_float(t[0]) + __uint_as_float(t[1]);
}
__device__ __forceinline__ float xrow_max(float x) {
    auto s = __builtin_amdgcn_permlane16_swap(__float_as_uint(x), __float_as_uint(x), false, false);
    x = fmaxf(__uint_as_float(s[0]), __uint_as_float(s[1]));
    auto t = __builtin_amdgcn_permlane32_swap(__float_as_uint(x), __float_as_uint(x), false, false);
    return fmaxf(__uint_as_float(t[0]), __uint_as_float(t[1]));
}
__device__ __forceinline__ float wsum(float v) {
    v += dppf<XOR1>(v); v += dppf<XOR2>(v); v += dppf<HMIR>(v); v += dppf<MIR>(v);
    return xrow_sum(v);
}
__device__ __forceinline__ void barrier_raw() {
    asm volatile("s_waitcnt lgkmcnt(0)" ::: "memory");
    __builtin_amdgcn_s_barrier();
    asm volatile("" ::: "memory");
}
typedef __amdgpu_buffer_rsrc_t rsrc_t;
__device__ __forceinline__ void put2(rsrc_t rs, u32 off, u32 tag, u32 v0, u32 v1) {
    const u32x4 d = {v0, tag, v1, tag};
    __builtin_amdgcn_raw_buffer_store_b128(d, rs, (int)off, 0, 16  );
}
template <int N>
__device__ __forceinline__ void sweep(rsrc_t rs, const u32 (&off)[N], u32 tag, u32x4 (&v)[N]) {
    for (unsigned spins = 0; spins < SPIN_CAP; ++spins) {
        asm volatile("" ::: "memory");
        bool ok = true;
#pragma unroll
        for (int k = 0; k < N; ++k) v[k] = __builtin_amdgcn_raw_buffer_load_b128(rs, (int)off[k], 0, 16  );
#pragma unroll
        for (int k = 0; k < N; ++k) ok &= (v[k][1] == tag) & (v[k][3] == tag);
        if (__all(ok)) return;
    }
}

__device__ __forceinline__ u32x4 ln_row(u32x4 xr) {
    float v[8];
#pragma unroll
    for (int i = 0; i < 4; ++i) { v[2 * i] = lo2f(xr[i]); v[2 * i + 1] = hi2f(xr[i]); }
    const float s = ((v[0] + v[1]) + (v[2] + v[3])) + ((v[4] + v[5]) + (v[6] + v[7]));
    const float mean = wsum(s) * (1.0f / 512.0f);
    float s2 = 0.f;
#pragma unroll
    for (int i = 0; i < 8; ++i) { const float d = v[i] - mean; s2 += d * d; }
    const float var = wsum(s2) * (1.0f / 512.0f);
    const float mb = bfr(mean);
    const float den = bfr(__builtin_amdgcn_sqrtf(bfr(bfr(var) + 1e-5f)));
    const float inv = __builtin_amdgcn_rcpf(den);
    u32x4 out;
#pragma unroll
    for (int i = 0; i < 4; ++i) {
        const u32 d = pk(v[2 * i] - mb, v[2 * i + 1] - mb);
        out[i] = pk(lo2f(d) * inv, hi2f(d) * inv);
    }
    return out;
}

struct __attribute__((aligned(16))) Lds {
    float ow[8][512];
    float mw[8][4], lw[8][4];
    float gupart[8][64];
    u16 ln[2][512];
    u16 qs[512];
    u16 os[32];
    u16 hh[2][16];
    u32 tag;
};

__global__ void __launch_bounds__(NTHR) mega(const u16* x, const u16* kc, const u16* vc, const u16* wq, const u16* wo,
                                             const u16* wg, const u16* wu, const u16* wd, u16* out, u32* wsg) {
    __shared__ Lds L;
    const int tid = threadIdx.x, lane = tid & 63;
    const int w = __builtin_amdgcn_readfirstlane(tid >> 6);
    const int bid = blockIdx.x;
    const int kvh = bid & 7, r = bid >> 3, b = r >> 4, s = r & 15;
    const bool is_mlp = r < 14;
    const int mblk = kvh * 14 + r;
    const int mc0 = mblk * 16;
    const rsrc_t G = __builtin_amdgcn_make_buffer_rsrc((void*)wsg, 0, (int)O_END, 0x00020000);
    const int g = lane >> 4, li = lane & 15;
    u32x4 sel[2];
#pragma unroll
    for (int h = 0; h < 2; ++h) {
        const bool on = (g == 2 * h + (li >> 3));
        const u32 one = ((li & 1) ? 0x3F800000u : 0x00003F80u);
#pragma unroll
        for (int i = 0; i < 4; ++i) sel[h][i] = (on && ((li & 7) >> 1) == i) ? one : 0u;
    }
    const f32x4 zero4 = {0.f, 0.f, 0.f, 0.f};

    if (w < 8) {
        u32x4 kr[2][4], vr[2][4], wor[2][2], gur[4], wdr[2];
        __builtin_amdgcn_s_barrier();
        asm volatile("" ::: "memory");
        const size_t kvoff = ((size_t)(b * 8 + kvh) * SK + (SK - WIN) + s * 256 + w * 32) * 128;
#pragma unroll
        for (int t = 0; t < 2; ++t)
#pragma unroll
            for (int kk = 0; kk < 4; ++kk) kr[t][kk] = ldg(kc + kvoff + (size_t)(t * 16 + li) * 128 + kk * 32 + g * 8);
        asm volatile("" ::: "memory");
#pragma unroll
        for (int t = 0; t < 2; ++t)
#pragma unroll
            for (int kk = 0; kk < 4; ++kk) vr[t][kk] = ldg(vc + kvoff + (size_t)(t * 16 + li) * 128 + kk * 32 + g * 8);
        asm volatile("" ::: "memory");
#pragma unroll
        for (int t = 0; t < 2; ++t)
#pragma unroll
            for (int nc = 0; nc < 2; ++nc)
                wor[t][nc] = ldc(wo + (size_t)(kvh * 512 + s * 32 + t * 16 + li) * 512 + w * 64 + nc * 32 + g * 8);
        if (is_mlp) {
            const u16* gu = (g < 2) ? wg : wu;
#pragma unroll
            for (int t = 0; t < 4; ++t) gur[t] = ldc(gu + (size_t)(w * 64 + t * 16 + li) * FF + mc0 + (g & 1) * 8);
#pragma unroll
            for (int nc = 0; nc < 2; ++nc) wdr[nc] = ldg(wd + (size_t)(mc0 + li) * 512 + w * 64 + nc * 32 + g * 8);
        }
        CSTAMP(12);
        barrier_raw();
        CSTAMP(13);

        {
            f32x4 st[2] = {zero4, zero4};
#pragma unroll
            for (int kk = 0; kk < 4; ++kk) {
                const u32x4 qf = *(const u32x4*)&L.qs[(li & 3) * 128 + kk * 32 + g * 8];
                st[0] = mfma(kr[0][kk], qf, st[0]);
                st[1] = mfma(kr[1][kk], qf, st[1]);
            }
            CSTAMP(14);
            float sc[8];
#pragma unroll
            for (int t = 0; t < 2; ++t)
#pragma unroll
                for (int i = 0; i < 2; ++i) {
                    const u32 a = pk(st[t][2 * i], st[t][2 * i + 1]);
                    const u32 c = pk(lo2f(a) * 0.08838834764831845f, hi2f(a) * 0.08838834764831845f);
                    sc[t * 4 + 2 * i] = lo2f(c); sc[t * 4 + 2 * i + 1] = hi2f(c);
                }
            float m = fmaxf(fmaxf(fmaxf(sc[0], sc[1]), fmaxf(sc[2], sc[3])), fmaxf(fmaxf(sc[4], sc[5]), fmaxf(sc[6], sc[7])));
            m = xrow_max(m);
            float p[8], l = 0.f;
#pragma unroll
            for (int i = 0; i < 8; ++i) { p[i] = __expf(sc[i] - m); l += p[i]; }
            l = xrow_sum(l);
            CSTAMP(15);
            u32x4 pf;
#pragma unroll
            for (int i = 0; i < 4; ++i) pf[i] = pk(p[2 * i], p[2 * i + 1]);
#pragma unroll
            for (int dt = 0; dt < 8; ++dt) {
                const f32x4 t0 = mfma(vr[0][dt >> 1], sel[dt & 1], zero4);
                const f32x4 t1 = mfma(vr[1][dt >> 1], sel[dt & 1], zero4);
                const f32x4 o = mfma(pack2(t0, t1), pf, zero4);
                *(f32x4*)&L.ow[w][(li & 3) * 128 + dt * 16 + g * 4] = o;
            }
            if (lane < 4) { L.mw[w][lane] = m; L.lw[w][lane] = l; }
        }
        CSTAMP(16);
        barrier_raw();
        CSTAMP(17);
        const u32 tag = L.tag;
        if (w < 4) {
            const int head = w;
            float M = L.mw[0][head];
#pragma unroll
            for (int j = 1; j < 8; ++j) M = fmaxf(M, L.mw[j][head]);
            float o0 = 0.f, o1 = 0.f, ls = 0.f;
#pragma unroll
            for (int j = 0; j < 8; ++j) {
                const float f = __expf(L.mw[j][head] - M);
                const float2 ov = *(const float2*)&L.ow[j][head * 128 + 2 * lane];
                o0 += f * ov.x; o1 += f * ov.y;
                ls += f * L.lw[j][head];
            }
            const u32 p1 = (kvh * 2 + b) * 16 + s;
            put2(G, O_1O + (p1 * 256 + head * 64 + lane) * 16, tag, __float_as_uint(o0), __float_as_uint(o1));
            if (lane == 0) put2(G, O_1ML + (p1 * 4 + head) * 16, tag, __float_as_uint(M), __float_as_uint(ls));
        }
        CSTAMP(18);
        u32x4 woT[4], guT[4], wdT[4];
#pragma unroll
        for (int T = 0; T < 4; ++T)
            woT[T] = pack2(mfma(wor[0][T >> 1], sel[T & 1], zero4), mfma(wor[1][T >> 1], sel[T & 1], zero4));
        if (is_mlp) {
#pragma unroll
            for (int pp = 0; pp < 2; ++pp)
#pragma unroll
                for (int mt = 0; mt < 2; ++mt)
                    guT[pp * 2 + mt] = pack2(mfma(gur[2 * pp], sel[mt], zero4), mfma(gur[2 * pp + 1], sel[mt], zero4));
#pragma unroll
            for (int T = 0; T < 4; ++T) wdT[T] = pack2(mfma(wdr[T >> 1], sel[T & 1], zero4), zero4);
        }
        barrier_raw();
        CSTAMP(19);
        {
            const u32x2 a0 = *(const u32x2*)&L.os[g * 4];
            const u32x2 a1 = *(const u32x2*)&L.os[16 + g * 4];
            const u32x4 bf = {a0[0], a0[1], a1[0], a1[1]};
            float v0 = 0.f, v1 = 0.f;
#pragma unroll
            for (int T = 0; T < 4; ++T) {
                const f32x4 d = mfma(woT[T], bf, zero4);
                const bool mine = (li >> 1) == T;
                v0 = mine ? ((li & 1) ? d[2] : d[0]) : v0;
                v1 = mine ? ((li & 1) ? d[3] : d[1]) : v1;
            }
            if (li < 8)
                put2(G, O_2 + ((b * 128 + kvh * 16 + s) * 256 + w * 32 + (li >> 1) * 8 + g * 2 + (li & 1)) * 16, tag,
                     __float_as_uint(v0), __float_as_uint(v1));
        }
        CSTAMP(20);
        if (is_mlp) {
            barrier_raw();
            CSTAMP(21);
            f32x4 ag = zero4, au = zero4;
#pragma unroll
            for (int pp = 0; pp < 2; ++pp) {
                const u32x2 a0 = *(const u32x2*)&L.ln[li & 1][w * 64 + pp * 32 + g * 4];
                const u32x2 a1 = *(const u32x2*)&L.ln[li & 1][w * 64 + pp * 32 + 16 + g * 4];
                const u32x4 bf = {a0[0], a0[1], a1[0], a1[1]};
                ag = mfma(guT[pp * 2], bf, ag);
                au = mfma(guT[pp * 2 + 1], bf, au);
            }
            if (li < 2) {
                *(f32x4*)&L.gupart[w][li * 16 + g * 4] = ag;
                *(f32x4*)&L.gupart[w][32 + li * 16 + g * 4] = au;
            }
            CSTAMP(23);
            barrier_raw();
            barrier_raw();
            CSTAMP(24);
            {
                const u32x2 a0 = *(const u32x2*)&L.hh[li & 1][g * 4];
                const u32x4 bf = {a0[0], a0[1], 0u, 0u};
                float v0 = 0.f, v1 = 0.f;
                const int rep = li >> 1;
#pragma unroll
                for (int T = 0; T < 4; ++T) {
                    const f32x4 d = mfma(wdT[T], bf, zero4);
                    const bool mine = (rep >> 1) == T;
                    v0 = mine ? ((rep & 1) ? d[2] : d[0]) : v0;
                    v1 = mine ? ((rep & 1) ? d[3] : d[1]) : v1;
                }
                put2(G, O_4 + (mblk * 512 + (li & 1) * 256 + w * 32 + (rep >> 1) * 8 + g * 2 + (rep & 1)) * 16, tag,
                     __float_as_uint(v0), __float_as_uint(v1));
            }
            CSTAMP(25);
        }
    } else {
        u64 tst[10];
        STAMP(0);
        const u32 ep = __hip_atomic_load((gu32*)(wsg), RLX_AGENT);
        const int rb = bid >> 7, rc = (bid & 127) * 4 + (lane & 1) * 2;
        const u32 xpair = *(const u32*)(x + rb * 512 + rc);
        u32x4 xr[2], wqr[16];
#pragma unroll
        for (int bb = 0; bb < 2; ++bb) xr[bb] = ldg(x + bb * 512 + lane * 8);
        {
            const u16* wp = wq + (size_t)((g >> 1) * 32 + li) * 4096 + kvh * 512 + r * 16 + (g & 1) * 8;
#pragma unroll
            for (int j = 0; j < 16; ++j) wqr[j] = ldc(wp + (size_t)((j >> 1) * 64 + (j & 1) * 16) * 4096);
        }
        asm volatile("" ::: "memory");
        __builtin_amdgcn_s_barrier();
        asm volatile("" ::: "memory");
        __builtin_amdgcn_sched_barrier(0);
        const u32 tag = ep + 1u;
        if (lane == 0) L.tag = tag;
        *(u32x4*)&L.ln[0][lane * 8] = ln_row(xr[0]);
        *(u32x4*)&L.ln[1][lane * 8] = ln_row(xr[1]);
        asm volatile("s_waitcnt lgkmcnt(0)" ::: "memory");
        STAMP(1);
        {
            f32x4 acc0 = zero4, acc1 = zero4;
#pragma unroll
            for (int jp = 0; jp < 8; ++jp) {
#pragma unroll
                for (int h = 0; h < 2; ++h) {
                    const u32x2 a0 = *(const u32x2*)&L.ln[li & 1][jp * 64 + h * 32 + g * 4];
                    const u32x2 a1 = *(const u32x2*)&L.ln[li & 1][jp * 64 + h * 32 + 16 + g * 4];
                    const u32x4 bf = {a0[0], a0[1], a1[0], a1[1]};
                    const u32x4 a2 = pack2(mfma(wqr[2 * jp], sel[h], zero4), mfma(wqr[2 * jp + 1], sel[h], zero4));
                    if (h == 0) acc0 = mfma(a2, bf, acc0); else acc1 = mfma(a2, bf, acc1);
                }
            }
            const u32 p01 = pk(acc0[0] + acc1[0], acc0[1] + acc1[1]), p23 = pk(acc0[2] + acc1[2], acc0[3] + acc1[3]);
            if (li < 2) put2(G, O_Q + ((kvh * 2 + li) * 128 + r * 4 + g) * 16, tag, p01, p23);
        }
        {
            u32 off[2]; u32x4 v[2];
#pragma unroll
            for (int k = 0; k < 2; ++k) off[k] = O_Q + ((kvh * 2 + b) * 128 + k * 64 + lane) * 16;
            sweep<2>(G, off, tag, v);
#pragma unroll
            for (int k = 0; k < 2; ++k) *(u32x2*)&L.qs[(k * 64 + lane) * 4] = u32x2{v[k][0], v[k][2]};
        }
        STAMP(2);
        barrier_raw();
        barrier_raw();
        STAMP(3);
        {
            const u32 head = s >> 2, grp = (kvh * 2 + b) * 16;
            u32 off[5]; u32x4 v[5];
#pragma unroll
            for (int i = 0; i < 4; ++i) off[i] = O_1O + ((grp + 4 * i + g) * 256 + s * 16 + li) * 16;
            off[4] = O_1ML + ((grp + li) * 4 + head) * 16;
            sweep<5>(G, off, tag, v);
            const float mj = __uint_as_float(v[4][0]), lj = __uint_as_float(v[4][2]);
            float M = fmaxf(mj, dppf<XOR1>(mj)); M = fmaxf(M, dppf<XOR2>(M)); M = fmaxf(M, dppf<HMIR>(M)); M = fmaxf(M, dppf<MIR>(M));
            const float wj = __expf(mj - M);
            float ls = wj * lj;
            ls += dppf<XOR1>(ls); ls += dppf<XOR2>(ls); ls += dppf<HMIR>(ls); ls += dppf<MIR>(ls);
            float o0 = 0.f, o1 = 0.f;
#pragma unroll
            for (int i = 0; i < 4; ++i) {
                const float w0 = __builtin_bit_cast(float, __builtin_amdgcn_readlane(__builtin_bit_cast(int, wj), 4 * i));
                const float w1 = __builtin_bit_cast(float, __builtin_amdgcn_readlane(__builtin_bit_cast(int, wj), 4 * i + 1));
                const float w2 = __builtin_bit_cast(float, __builtin_amdgcn_readlane(__builtin_bit_cast(int, wj), 4 * i + 2));
                const float w3 = __builtin_bit_cast(float, __builtin_amdgcn_readlane(__builtin_bit_cast(int, wj), 4 * i + 3));
                const float ww = (g & 2) ? ((g & 1) ? w3 : w2) : ((g & 1) ? w1 : w0);
                o0 += ww * __uint_as_float(v[i][0]);
                o1 += ww * __uint_as_float(v[i][2]);
            }
            o0 = xrow_sum(o0); o1 = xrow_sum(o1);
            const float inv = __builtin_amdgcn_rcpf(ls);
            if (lane < 16) ((u32*)L.os)[lane] = pk(o0 * inv, o1 * inv);
        }
        STAMP(4);
        barrier_raw();
        float h0, h1;
        {
            u32 off[4]; u32x4 v[4];
#pragma unroll
            for (int i = 0; i < 4; ++i) off[i] = O_2 + ((rb * 128 + i * 32 + (lane >> 1)) * 256 + (rc >> 1)) * 16;
            sweep<4>(G, off, tag, v);
            float s0 = 0.f, s1 = 0.f;
#pragma unroll
            for (int i = 0; i < 4; ++i) { s0 += __uint_as_float(v[i][0]); s1 += __uint_as_float(v[i][2]); }
            s0 += dppf<XOR2>(s0); s0 += dppf<ROR4>(s0); s0 += dppf<ROR8>(s0); s0 = xrow_sum(s0);
            s1 += dppf<XOR2>(s1); s1 += dppf<ROR4>(s1); s1 += dppf<ROR8>(s1); s1 = xrow_sum(s1);
            const u32 a = pk(s0, s1);
            const u32 pr = pk(lo2f(xpair) + lo2f(a), hi2f(xpair) + hi2f(a));
            h0 = lo2f(pr); h1 = hi2f(pr);
            const u32 pr2 = dppu<XOR1>(pr);
            if (lane == 0) put2(G, O_3 + bid * 16, tag, pr, pr2);
        }
        STAMP(5);
        if (is_mlp) {
            u32 off[4]; u32x4 v[4];
#pragma unroll
            for (int i = 0; i < 4; ++i) off[i] = O_3 + (i * 64 + lane) * 16;
            sweep<4>(G, off, tag, v);
            STAMP(6);
#pragma unroll
            for (int bb = 0; bb < 2; ++bb) {
                const u32x4 hr = {v[2 * bb][0], v[2 * bb][2], v[2 * bb + 1][0], v[2 * bb + 1][2]};
                const u32x4 ln = ln_row(hr);
                *(u32x2*)&L.ln[bb][lane * 4] = u32x2{ln[0], ln[1]};
                *(u32x2*)&L.ln[bb][256 + lane * 4] = u32x2{ln[2], ln[3]};
            }
            barrier_raw();
            barrier_raw();
            STAMP(7);
            {
                float a = 0.f;
#pragma unroll
                for (int j = 0; j < 8; ++j) a += L.gupart[j][lane];
                a = bfr(a);
                auto t = __builtin_amdgcn_permlane32_swap(__float_as_uint(a), __float_as_uint(a), false, false);
                const float u = __uint_as_float(t[1]);
                const float sg = bfr(__builtin_amdgcn_rcpf(1.0f + __expf(-a)));
                const float hv = bfr(sg * u);
                if (lane < 32) L.hh[lane >> 4][lane & 15] = (u16)(__float_as_uint(hv) >> 16);
            }
            barrier_raw();
        }
        STAMP(8);
        {
            u32 off[4]; u32x4 v[4];
#pragma unroll
            for (int i = 0; i < 4; ++i) {
                const u32 pidx = i * 32 + (lane >> 1);
                off[i] = O_4 + ((pidx < NMLP ? pidx : pidx - 16) * 512 + rb * 256 + (rc >> 1)) * 16;
            }
            sweep<4>(G, off, tag, v);
            float s0 = 0.f, s1 = 0.f;
#pragma unroll
            for (int i = 0; i < 4; ++i) {
                const bool live = (i * 32 + (lane >> 1)) < NMLP;
                s0 += live ? __uint_as_float(v[i][0]) : 0.f;
                s1 += live ? __uint_as_float(v[i][2]) : 0.f;
            }
            s0 += dppf<XOR2>(s0); s0 += dppf<ROR4>(s0); s0 += dppf<ROR8>(s0); s0 = xrow_sum(s0);
            s1 += dppf<XOR2>(s1); s1 += dppf<ROR4>(s1); s1 += dppf<ROR8>(s1); s1 = xrow_sum(s1);
            const u32 a = pk(s0, s1);
            const u32 res = pk(h0 + lo2f(a), h1 + hi2f(a));
            if (lane < 2) *(u32*)(out + rb * 512 + rc) = res;
        }
        STAMP(9);
        if (bid == 0 && lane == 0) __hip_atomic_store((gu32*)(wsg), tag, RLX_AGENT);
        if (lane == 0 && (bid % 16) == 3) printf("blk %3d t0 %llu : %d %d %d %d %d %d\n", bid, tst[0] % 100000000ull, (int)(tst[1]-tst[0]), (int)(tst[2]-tst[0]), (int)(tst[3]-tst[0]), (int)(tst[4]-tst[0]), (int)(tst[5]-tst[0]), (int)(tst[9]-tst[0]));
    }
}

extern "C" void kernel_launch(void* const* d_in, const int* in_sizes, int n_in,
                              void* d_out, int out_size, void* d_ws, size_t ws_size,
                              hipStream_t stream) {
    mega<<<NBLK, NTHR, 0, stream>>>((const u16*)d_in[0], (const u16*)d_in[1], (const u16*)d_in[2], (const u16*)d_in[3],
                                    (const u16*)d_in[4], (const u16*)d_in[6], (const u16*)d_in[7], (const u16*)d_in[8],
                                    (u16*)d_out, (u32*)d_ws);
}
```

```cpp
#include <hip/hip_runtime.h>
#include <stdint.h>

typedef unsigned short u16;
typedef unsigned int u32;
typedef unsigned long long u64;
typedef __attribute__((ext_vector_type(4))) unsigned u32x4;
typedef __attribute__((ext_vector_type(2))) unsigned u32x2;
typedef __attribute__((ext_vector_type(8))) short s16x8;
typedef __attribute__((ext_vector_type(4))) float f32x4;
typedef __attribute__((ext_vector_type(2))) __bf16 bf16x2;
#define GAS __attribute__((address_space(1)))
typedef GAS u32 gu32;
#define RLX_AGENT __ATOMIC_RELAXED, __HIP_MEMORY_SCOPE_AGENT

constexpr int SK = 32768, WIN = 4096, FF = 1792;
constexpr int NBLK = 256, NTHR = 576, NMLP = 112;
constexpr unsigned SPIN_CAP = 1u << 18;

constexpr u32 O_EPOCH = 0;
constexpr u32 O_Q = 256;
constexpr u32 O_1O = O_Q + 8 * 2 * 128 * 16;
constexpr u32 O_1ML = O_1O + 256 * 256 * 16;
constexpr u32 O_2 = O_1ML + 256 * 4 * 16;
constexpr u32 O_3 = O_2 + 256 * 256 * 16;
constexpr u32 O_4 = O_3 + 256 * 16;
constexpr u32 O_END = O_4 + NMLP * 512 * 16;

#define STAMP(i) do { } while (0)
#define CSTAMP(i) do { } while (0)

__device__ __forceinline__ float lo2f(u32 v) { return __uint_as_float(v << 16); }
__device__ __forceinline__ float hi2f(u32 v) { return __uint_as_float(v & 0xffff0000u); }
__device__ __forceinline__ u32 pk(float lo, float hi) {
    const bf16x2 v = {(__bf16)lo, (__bf16)hi};
    return __builtin_bit_cast(u32, v);
}
__device__ __forceinline__ float bfr(float f) { return lo2f(pk(f, f)); }
__device__ __forceinline__ u32 packhi(float lo, float hi) {
    return __builtin_amdgcn_perm(__float_as_uint(hi), __float_as_uint(lo), 0x07060302u);
}
__device__ __forceinline__ u32x4 ldg(const u16* p) { return __builtin_nontemporal_load((const u32x4*)p); }
__device__ __forceinline__ u32x4 ldc(const u16* p) { return *(const u32x4*)p; }
__device__ __forceinline__ f32x4 mfma(u32x4 a, u32x4 b, f32x4 c) {
    return __builtin_amdgcn_mfma_f32_16x16x32_bf16(__builtin_bit_cast(s16x8, a), __builtin_bit_cast(s16x8, b), c, 0, 0, 0);
}
__device__ __forceinline__ u32x4 pack2(f32x4 t0, f32x4 t1) {
    u32x4 r;
    r[0] = packhi(t0[0], t0[1]); r[1] = packhi(t0[2], t0[3]);
    r[2] = packhi(t1[0], t1[1]); r[3] = packhi(t1[2], t1[3]);
    return r;
}
template <int CTRL> __device__ __forceinline__ float dppf(float x) {
    return __builtin_bit_cast(float, __builtin_amdgcn_update_dpp(0, __builtin_bit_cast(int, x), CTRL, 0xf, 0xf, true));
}
template <int CTRL> __device__ __forceinline__ u32 dppu(u32 x) {
    return (u32)__builtin_amdgcn_update_dpp(0, (int)x, CTRL, 0xf, 0xf, true);
}
constexpr int XOR1 = 0xB1, XOR2 = 0x4E, HMIR = 0x141, MIR = 0x140, ROR4 = 0x124, ROR8 = 0x128;
__device__ __forceinline__ float xrow_sum(float x) {
    auto s = __builtin_amdgcn_permlane16_swap(__float_as_uint(x), __float_as_uint(x), false, false);
    x = __uint_as_float(s[0]) + __uint_as_float(s[1]);
    auto t = __builtin_amdgcn_permlane32_swap(__float_as_uint(x), __float_as_uint(x), false, false);
    return __uint_as_float(t[0]) + __uint_as_float(t[1]);
}
__device__ __forceinline__ float xrow_max(float x) {
    auto s = __builtin_amdgcn_permlane16_swap(__float_as_uint(x), __float_as_uint(x), false, false);
    x = fmaxf(__uint_as_float(s[0]), __uint_as_float(s[1]));
    auto t = __builtin_amdgcn_permlane32_swap(__float_as_uint(x), __float_as_uint(x), false, false);
    return fmaxf(__uint_as_float(t[0]), __uint_as_float(t[1]));
}
__device__ __forceinline__ float wsum(float v) {
    v += dppf<XOR1>(v); v += dppf<XOR2>(v); v += dppf<HMIR>(v); v += dppf<MIR>(v);
    return xrow_sum(v);
}
__device__ __forceinline__ void barrier_raw() {
    asm volatile("s_waitcnt lgkmcnt(0)" ::: "memory");
    __builtin_amdgcn_s_barrier();
    asm volatile("" ::: "memory");
}
typedef __amdgpu_buffer_rsrc_t rsrc_t;
#define LAS __attribute__((address_space(3)))
__device__ __forceinline__ void glds16(const u16* gsrc, u32 lds_dst) {
    u32 keep;
    asm volatile("s_mov_b32 %0, m0\n\ts_mov_b32 m0, %2\n\ts_nop 0\n\tglobal_load_lds_dwordx4 %1, off nt\n\ts_mov_b32 m0, %0"
                 : "=&s"(keep) : "v"(gsrc), "s"(lds_dst) : "memory");
}
__device__ __forceinline__ void put2(rsrc_t rs, u32 off, u32 tag, u32 v0, u32 v1) {
    const u32x4 d = {v0, tag, v1, tag};
    __builtin_amdgcn_raw_buffer_store_b128(d, rs, (int)off, 0, 16  );
}
template <int N>
__device__ __forceinline__ void sweep(rsrc_t rs, const u32 (&off)[N], u32 tag, u32x4 (&v)[N]) {
    for (unsigned spins = 0; spins < SPIN_CAP; ++spins) {
        asm volatile("" ::: "memory");
        bool ok = true;
#pragma unroll
        for (int k = 0; k < N; ++k) v[k] = __builtin_amdgcn_raw_buffer_load_b128(rs, (int)off[k], 0, 16  );
#pragma unroll
        for (int k = 0; k < N; ++k) ok &= (v[k][1] == tag) & (v[k][3] == tag);
        if (__all(ok)) return;
    }
}

__device__ __forceinline__ u32x4 ln_row(u32x4 xr) {
    float v[8];
#pragma unroll
    for (int i = 0; i < 4; ++i) { v[2 * i] = lo2f(xr[i]); v[2 * i + 1] = hi2f(xr[i]); }
    const float s = ((v[0] + v[1]) + (v[2] + v[3])) + ((v[4] + v[5]) + (v[6] + v[7]));
    const float mean = wsum(s) * (1.0f / 512.0f);
    float s2 = 0.f;
#pragma unroll
    for (int i = 0; i < 8; ++i) { const float d = v[i] - mean; s2 += d * d; }
    const float var = wsum(s2) * (1.0f / 512.0f);
    const float mb = bfr(mean);
    const float den = bfr(__builtin_amdgcn_sqrtf(bfr(bfr(var) + 1e-5f)));
    const float inv = __builtin_amdgcn_rcpf(den);
    u32x4 out;
#pragma unroll
    for (int i = 0; i < 4; ++i) {
        const u32 d = pk(v[2 * i] - mb, v[2 * i + 1] - mb);
        out[i] = pk(lo2f(d) * inv, hi2f(d) * inv);
    }
    return out;
}

struct __attribute__((aligned(16))) Lds {
    u16 kbuf[8][32 * 128];
    u16 pl[8][4][4][8];
    float ow[8][512];
    float mw[8][4], lw[8][4];
    float gupart[8][64];
    u16 ln[2][512];
    u16 qs[512];
    u16 os[32];
    u16 hh[2][16];
    u32 tag;
};

__global__ void __launch_bounds__(NTHR) mega(const u16* x, const u16* kc, const u16* vc, const u16* wq, const u16* wo,
                                             const u16* wg, const u16* wu, const u16* wd, u16* out, u32* wsg) {
    __shared__ Lds L;
    const int tid = threadIdx.x, lane = tid & 63;
    const int w = __builtin_amdgcn_readfirstlane(tid >> 6);
    const int bid = blockIdx.x;
    const int kvh = bid & 7, r = bid >> 3, b = r >> 4, s = r & 15;
    const bool is_mlp = r < 14;
    const int mblk = kvh * 14 + r;
    const int mc0 = mblk * 16;
    const rsrc_t G = __builtin_amdgcn_make_buffer_rsrc((void*)wsg, 0, (int)O_END, 0x00020000);
    const int g = lane >> 4, li = lane & 15;
    u32x4 sel[2];
#pragma unroll
    for (int h = 0; h < 2; ++h) {
        const bool on = (g == 2 * h + (li >> 3));
        const u32 one = ((li & 1) ? 0x3F800000u : 0x00003F80u);
#pragma unroll
        for (int i = 0; i < 4; ++i) sel[h][i] = (on && ((li & 7) >> 1) == i) ? one : 0u;
    }
    const f32x4 zero4 = {0.f, 0.f, 0.f, 0.f};

    if (w < 8) {
        u32x4 vr[8], wor[4], gur[4], wdr[2];
        __builtin_amdgcn_s_barrier();
        asm volatile("" ::: "memory");
        const size_t kvoff = ((size_t)(b * 8 + kvh) * SK + (SK - WIN) + s * 256 + w * 32) * 128;
        {
            const u32 kdst = (u32)(uintptr_t)(LAS u16*)&L.kbuf[w][0];
#pragma unroll
            for (int j = 0; j < 8; ++j)
                glds16(kc + kvoff + (size_t)(4 * j + g) * 128 + ((li ^ (4 * (j & 3) + g)) * 8), kdst + j * 1024);
        }
        __builtin_amdgcn_s_barrier();
        asm volatile("" ::: "memory");
#pragma unroll
        for (int j = 0; j < 8; ++j) vr[j] = ldg(vc + kvoff + (size_t)j * 512 + lane * 8);
        asm volatile("" ::: "memory");
        __builtin_amdgcn_s_barrier();
        asm volatile("" ::: "memory");
#pragma unroll
        for (int j = 0; j < 4; ++j)
            wor[j] = ldg(wo + (size_t)(kvh * 512 + s * 32 + 8 * j + (lane >> 3)) * 512 + w * 64 + (lane & 7) * 8);
        {
            const u16* gu = is_mlp ? ((g < 2) ? wg : wu) + (size_t)(w * 64 + li) * FF + mc0 + (g & 1) * 8 : wo;
            const size_t gstep = is_mlp ? (size_t)16 * FF : 0;
#pragma unroll
            for (int t = 0; t < 4; ++t) gur[t] = ldc(gu + t * gstep);
            const u16* dp = is_mlp ? wd + (size_t)(mc0 + (lane >> 3)) * 512 + w * 64 + (lane & 7) * 8 : wo;
#pragma unroll
            for (int j = 0; j < 2; ++j) wdr[j] = ldg(dp + (is_mlp ? j * 8 * 512 : 0));
        }
        asm volatile("" ::: "memory");
        u32 mk[4];
#pragma unroll
        for (int d = 0; d < 4; ++d) mk[d] = (((li & 7) >> 1) == d) ? 0xFFFFFFFFu : 0u;
        const u32 sh = 16 * (li & 1);
        CSTAMP(12);
        barrier_raw();
        CSTAMP(13);

        {
            asm volatile("s_waitcnt vmcnt(18)" ::: "memory");
            f32x4 st[2] = {zero4, zero4};
#pragma unroll
            for (int kk = 0; kk < 4; ++kk) {
                const u32x4 qf = *(const u32x4*)&L.qs[(li & 3) * 128 + kk * 32 + g * 8];
#pragma unroll
                for (int t = 0; t < 2; ++t) {
                    const u32x4 kf = *(const u32x4*)&L.kbuf[w][(t * 16 + li) * 128 + (((kk * 4 + g) ^ li) * 8)];
                    st[t] = mfma(kf, qf, st[t]);
                }
            }
            CSTAMP(14);
            float sc[8];
#pragma unroll
            for (int t = 0; t < 2; ++t)
#pragma unroll
                for (int i = 0; i < 2; ++i) {
                    const u32 a = pk(st[t][2 * i], st[t][2 * i + 1]);
                    const u32 c = pk(lo2f(a) * 0.08838834764831845f, hi2f(a) * 0.08838834764831845f);
                    sc[t * 4 + 2 * i] = lo2f(c); sc[t * 4 + 2 * i + 1] = hi2f(c);
                }
            float m = fmaxf(fmaxf(fmaxf(sc[0], sc[1]), fmaxf(sc[2], sc[3])), fmaxf(fmaxf(sc[4], sc[5]), fmaxf(sc[6], sc[7])));
            m = xrow_max(m);
            float p[8], l = 0.f;
#pragma unroll
            for (int i = 0; i < 8; ++i) { p[i] = __expf(sc[i] - m); l += p[i]; }
            l = xrow_sum(l);
#pragma unroll
            for (int t = 0; t < 2; ++t)
#pragma unroll
                for (int i = 0; i < 2; ++i) {
                    const u32 pp = pk(p[t * 4 + 2 * i], p[t * 4 + 2 * i + 1]);
                    L.pl[w][li & 3][2 * i][t * 4 + g] = (u16)(pp & 0xffffu);
                    L.pl[w][li & 3][2 * i + 1][t * 4 + g] = (u16)(pp >> 16);
                }
            if (lane < 4) { L.mw[w][lane] = m; L.lw[w][lane] = l; }
            asm volatile("s_waitcnt lgkmcnt(0)" ::: "memory");
            CSTAMP(15);
            u32x4 pv[2];
#pragma unroll
            for (int hp = 0; hp < 2; ++hp) pv[hp] = *(const u32x4*)&L.pl[w][2 * hp + (li >> 3)][g][0];
            f32x4 oacc[2] = {zero4, zero4};
#pragma unroll
            for (int j = 0; j < 8; ++j)
#pragma unroll
                for (int hp = 0; hp < 2; ++hp) {
                    const u32 xv = (j & 1) ? (pv[hp][j >> 1] >> 16) : (pv[hp][j >> 1] & 0xffffu);
                    const u32 val = xv << sh;
                    const u32x4 bf = {val & mk[0], val & mk[1], val & mk[2], val & mk[3]};
                    oacc[hp] = mfma(vr[j], bf, oacc[hp]);
                }
#pragma unroll
            for (int hp = 0; hp < 2; ++hp)
#pragma unroll
                for (int ii = 0; ii < 4; ++ii) L.ow[w][(2 * hp + (li >> 3)) * 128 + (g * 4 + ii) * 8 + (li & 7)] = oacc[hp][ii];
        }
        CSTAMP(16);
        barrier_raw();
        CSTAMP(17);
        const u32 tag = L.tag;
        if (w < 4) {
            const int head = w;
            float M = L.mw[0][head];
#pragma unroll
            for (int j = 1; j < 8; ++j) M = fmaxf(M, L.mw[j][head]);
            float o0 = 0.f, o1 = 0.f, ls = 0.f;
#pragma unroll
            for (int j = 0; j < 8; ++j) {
                const float f = __expf(L.mw[j][head] - M);
                const float2 ov = *(const float2*)&L.ow[j][head * 128 + 2 * lane];
                o0 += f * ov.x; o1 += f * ov.y;
                ls += f * L.lw[j][head];
            }
            const u32 p1 = (kvh * 2 + b) * 16 + s;
            put2(G, O_1O + (p1 * 256 + head * 64 + lane) * 16, tag, __float_as_uint(o0), __float_as_uint(o1));
            if (lane == 0) put2(G, O_1ML + (p1 * 4 + head) * 16, tag, __float_as_uint(M), __float_as_uint(ls));
        }
        CSTAMP(18);
        u32x4 guT[4];
#pragma unroll
        for (int pp = 0; pp < 2; ++pp)
#pragma unroll
            for (int mt = 0; mt < 2; ++mt)
                guT[pp * 2 + mt] = pack2(mfma(gur[2 * pp], sel[mt], zero4), mfma(gur[2 * pp + 1], sel[mt], zero4));
        asm volatile("" : "+v"(guT[0]), "+v"(guT[1]), "+v"(guT[2]), "+v"(guT[3]), "+v"(wor[0]), "+v"(wor[1]), "+v"(wor[2]), "+v"(wor[3]), "+v"(wdr[0]), "+v"(wdr[1]));
        barrier_raw();
        CSTAMP(19);
        const bool dvalid = (li >> 3) == (g >> 1);
        const int rep = (g >> 1) * 2 + (li >> 3);
        {
            f32x4 d = zero4;
#pragma unroll
            for (int j = 0; j < 4; ++j) {
                const u32 pr = ((const u32*)L.os)[4 * j + g];
                const u32 xv = (li >> 3) ? (pr >> 16) : (pr & 0xffffu);
                const u32 val = xv << sh;
                const u32x4 bf = {val & mk[0], val & mk[1], val & mk[2], val & mk[3]};
                d = mfma(wor[j], bf, d);
            }
            float y[4];
#pragma unroll
            for (int ii = 0; ii < 4; ++ii) {
                float v = dvalid ? d[ii] : 0.f;
                v += dppf<ROR8>(v);
                auto t = __builtin_amdgcn_permlane32_swap(__float_as_uint(v), __float_as_uint(v), false, false);
                y[ii] = __uint_as_float(t[0]) + __uint_as_float(t[1]);
            }
            const float v0 = (rep & 2) ? ((rep & 1) ? y[3] : y[2]) : ((rep & 1) ? y[1] : y[0]);
            const float v1 = dppf<XOR1>(v0);
            if ((li & 1) == 0)
                put2(G, O_2 + ((b * 128 + kvh * 16 + s) * 256 + w * 32 + ((g & 1) * 4 + rep) * 4 + ((li & 7) >> 1)) * 16, tag,
                     __float_as_uint(v0), __float_as_uint(v1));
        }
        CSTAMP(20);
        if (is_mlp) {
            barrier_raw();
            CSTAMP(21);
            f32x4 ag = zero4, au = zero4;
#pragma unroll
            for (int pp = 0; pp < 2; ++pp) {
                const u32x2 a0 = *(const u32x2*)&L.ln[li & 1][w * 64 + pp * 32 + g * 4];
                const u32x2 a1 = *(const u32x2*)&L.ln[li & 1][w * 64 + pp * 32 + 16 + g * 4];
                const u32x4 bf = {a0[0], a0[1], a1[0], a1[1]};
                ag = mfma(guT[pp * 2], bf, ag);
                au = mfma(guT[pp * 2 + 1], bf, au);
            }
            if (li < 2) {
                *(f32x4*)&L.gupart[w][li * 16 + g * 4] = ag;
                *(f32x4*)&L.gupart[w][32 + li * 16 + g * 4] = au;
            }
            CSTAMP(23);
            barrier_raw();
            barrier_raw();
            CSTAMP(24);
#pragma unroll
            for (int bb = 0; bb < 2; ++bb) {
                f32x4 d = zero4;
#pragma unroll
                for (int j = 0; j < 2; ++j) {
                    const u32 pr = ((const u32*)L.hh[bb])[4 * j + g];
                    const u32 xv = (li >> 3) ? (pr >> 16) : (pr & 0xffffu);
                    const u32 val = xv << sh;
                    const u32x4 bf = {val & mk[0], val & mk[1], val & mk[2], val & mk[3]};
                    d = mfma(wdr[j], bf, d);
                }
                float y[4];
#pragma unroll
                for (int ii = 0; ii < 4; ++ii) {
                    float v = dvalid ? d[ii] : 0.f;
                    v += dppf<ROR8>(v);
                    auto t = __builtin_amdgcn_permlane32_swap(__float_as_uint(v), __float_as_uint(v), false, false);
                    y[ii] = __uint_as_float(t[0]) + __uint_as_float(t[1]);
                }
                const float v0 = (rep & 2) ? ((rep & 1) ? y[3] : y[2]) : ((rep & 1) ? y[1] : y[0]);
                const float v1 = dppf<XOR1>(v0);
                if ((li & 1) == 0)
                    put2(G, O_4 + (mblk * 512 + bb * 256 + w * 32 + ((g & 1) * 4 + rep) * 4 + ((li & 7) >> 1)) * 16, tag,
                         __float_as_uint(v0), __float_as_uint(v1));
            }
            CSTAMP(25);
        }
    } else {
        STAMP(0);
        const u32 ep = __hip_atomic_load((gu32*)(wsg), RLX_AGENT);
        const int rb = bid >> 7, rc = (bid & 127) * 4 + (lane & 1) * 2;
        const u32 xpair = *(const u32*)(x + rb * 512 + rc);
        u32x4 xr[2], wqr[16];
#pragma unroll
        for (int bb = 0; bb < 2; ++bb) xr[bb] = ldg(x + bb * 512 + lane * 8);
        {
            const u16* wp = wq + (size_t)((g >> 1) * 32 + li) * 4096 + kvh * 512 + r * 16 + (g & 1) * 8;
#pragma unroll
            for (int j = 0; j < 16; ++j) wqr[j] = ldc(wp + (size_t)((j >> 1) * 64 + (j & 1) * 16) * 4096);
        }
        asm volatile("" ::: "memory");
        __builtin_amdgcn_s_barrier();
        asm volatile("" ::: "memory");
        __builtin_amdgcn_sched_barrier(0);
        const u32 tag = ep + 1u;
        if (lane == 0) L.tag = tag;
        *(u32x4*)&L.ln[0][lane * 8] = ln_row(xr[0]);
        *(u32x4*)&L.ln[1][lane * 8] = ln_row(xr[1]);
        asm volatile("s_waitcnt lgkmcnt(0)" ::: "memory");
        STAMP(1);
        {
            f32x4 acc0 = zero4, acc1 = zero4;
#pragma unroll
            for (int jp = 0; jp < 8; ++jp) {
#pragma unroll
                for (int h = 0; h < 2; ++h) {
                    const u32x2 a0 = *(const u32x2*)&L.ln[li & 1][jp * 64 + h * 32 + g * 4];
                    const u32x2 a1 = *(const u32x2*)&L.ln[li & 1][jp * 64 + h * 32 + 16 + g * 4];
                    const u32x4 bf = {a0[0], a0[1], a1[0], a1[1]};
                    const u32x4 a2 = pack2(mfma(wqr[2 * jp], sel[h], zero4), mfma(wqr[2 * jp + 1], sel[h], zero4));
                    if (h == 0) acc0 = mfma(a2, bf, acc0); else acc1 = mfma(a2, bf, acc1);
                }
            }
            const u32 p01 = pk(acc0[0] + acc1[0], acc0[1] + acc1[1]), p23 = pk(acc0[2] + acc1[2], acc0[3] + acc1[3]);
            STAMP(26);
            if (li < 2) put2(G, O_Q + ((kvh * 2 + li) * 128 + r * 4 + g) * 16, tag, p01, p23);
        }
        {
            u32 off[2]; u32x4 v[2];
#pragma unroll
            for (int k = 0; k < 2; ++k) off[k] = O_Q + ((kvh * 2 + b) * 128 + k * 64 + lane) * 16;
#pragma unroll
            for (int k = 0; k < 2; ++k) v[k] = __builtin_amdgcn_raw_buffer_load_b128(G, (int)off[k], 0, 16  );
            asm volatile("" ::: "memory");
            __builtin_amdgcn_s_barrier();
            __builtin_amdgcn_s_barrier();
            asm volatile("" ::: "memory");
            u32x4 v2[2];
#pragma unroll
            for (int k = 0; k < 2; ++k) v2[k] = __builtin_amdgcn_raw_buffer_load_b128(G, (int)off[k], 0, 16  );
            asm volatile("" ::: "memory");
            const bool ok = (v[0][1] == tag) & (v[0][3] == tag) & (v[1][1] == tag) & (v[1][3] == tag);
            STAMP(27);
            if (!__all(ok)) {
                STAMP(28);
                const bool ok2 = (v2[0][1] == tag) & (v2[0][3] == tag) & (v2[1][1] == tag) & (v2[1][3] == tag);
                v[0] = v2[0]; v[1] = v2[1];
                if (!__all(ok2)) { STAMP(29); sweep<2>(G, off, tag, v); }
            }
#pragma unroll
            for (int k = 0; k < 2; ++k) *(u32x2*)&L.qs[(k * 64 + lane) * 4] = u32x2{v[k][0], v[k][2]};
        }
        STAMP(2);
        barrier_raw();
        barrier_raw();
        STAMP(3);
        {
            const u32 head = s >> 2, grp = (kvh * 2 + b) * 16;
            u32 off[5]; u32x4 v[5];
#pragma unroll
            for (int i = 0; i < 4; ++i) off[i] = O_1O + ((grp + 4 * i + g) * 256 + s * 16 + li) * 16;
            off[4] = O_1ML + ((grp + li) * 4 + head) * 16;
            sweep<5>(G, off, tag, v);
            const float mj = __uint_as_float(v[4][0]), lj = __uint_as_float(v[4][2]);
            float M = fmaxf(mj, dppf<XOR1>(mj)); M = fmaxf(M, dppf<XOR2>(M)); M = fmaxf(M, dppf<HMIR>(M)); M = fmaxf(M, dppf<MIR>(M));
            const float wj = __expf(mj - M);
            float ls = wj * lj;
            ls += dppf<XOR1>(ls); ls += dppf<XOR2>(ls); ls += dppf<HMIR>(ls); ls += dppf<MIR>(ls);
            float o0 = 0.f, o1 = 0.f;
#pragma unroll
            for (int i = 0; i < 4; ++i) {
                const float w0 = __builtin_bit_cast(float, __builtin_amdgcn_readlane(__builtin_bit_cast(int, wj), 4 * i));
                const float w1 = __builtin_bit_cast(float, __builtin_amdgcn_readlane(__builtin_bit_cast(int, wj), 4 * i + 1));
                const float w2 = __builtin_bit_cast(float, __builtin_amdgcn_readlane(__builtin_bit_cast(int, wj), 4 * i + 2));
                const float w3 = __builtin_bit_cast(float, __builtin_amdgcn_readlane(__builtin_bit_cast(int, wj), 4 * i + 3));
                const float ww = (g & 2) ? ((g & 1) ? w3 : w2) : ((g & 1) ? w1 : w0);
                o0 += ww * __uint_as_float(v[i][0]);
                o1 += ww * __uint_as_float(v[i][2]);
            }
            o0 = xrow_sum(o0); o1 = xrow_sum(o1);
            const float inv = __builtin_amdgcn_rcpf(ls);
            if (lane < 16) ((u32*)L.os)[lane] = pk(o0 * inv, o1 * inv);
        }
        STAMP(4);
        barrier_raw();
        float h0, h1;
        {
            u32 off[4]; u32x4 v[4];
#pragma unroll
            for (int i = 0; i < 4; ++i) off[i] = O_2 + ((rb * 128 + i * 32 + (lane >> 1)) * 256 + (rc >> 1)) * 16;
            sweep<4>(G, off, tag, v);
            float s0 = 0.f, s1 = 0.f;
#pragma unroll
            for (int i = 0; i < 4; ++i) { s0 += __uint_as_float(v[i][0]); s1 += __uint_as_float(v[i][2]); }
            s0 += dppf<XOR2>(s0); s0 += dppf<ROR4>(s0); s0 += dppf<ROR8>(s0); s0 = xrow_sum(s0);
            s1 += dppf<XOR2>(s1); s1 += dppf<ROR4>(s1); s1 += dppf<ROR8>(s1); s1 = xrow_sum(s1);
            const u32 a = pk(s0, s1);
            const u32 pr = pk(lo2f(xpair) + lo2f(a), hi2f(xpair) + hi2f(a));
            h0 = lo2f(pr); h1 = hi2f(pr);
            const u32 pr2 = dppu<XOR1>(pr);
            if (lane == 0) put2(G, O_3 + bid * 16, tag, pr, pr2);
        }
        STAMP(5);
        if (is_mlp) {
            u32 off[4]; u32x4 v[4];
#pragma unroll
            for (int i = 0; i < 4; ++i) off[i] = O_3 + (i * 64 + lane) * 16;
            sweep<4>(G, off, tag, v);
            STAMP(6);
#pragma unroll
            for (int bb = 0; bb < 2; ++bb) {
                const u32x4 hr = {v[2 * bb][0], v[2 * bb][2], v[2 * bb + 1][0], v[2 * bb + 1][2]};
                const u32x4 ln = ln_row(hr);
                *(u32x2*)&L.ln[bb][lane * 4] = u32x2{ln[0], ln[1]};
                *(u32x2*)&L.ln[bb][256 + lane * 4] = u32x2{ln[2], ln[3]};
            }
            barrier_raw();
            barrier_raw();
            STAMP(7);
            {
                float a = 0.f;
#pragma unroll
                for (int j = 0; j < 8; ++j) a += L.gupart[j][lane];
                a = bfr(a);
                auto t = __builtin_amdgcn_permlane32_swap(__float_as_uint(a), __float_as_uint(a), false, false);
                const float u = __uint_as_float(t[1]);
                const float sg = bfr(__builtin_amdgcn_rcpf(1.0f + __expf(-a)));
                const float hv = bfr(sg * u);
                if (lane < 32) L.hh[lane >> 4][lane & 15] = (u16)(__float_as_uint(hv) >> 16);
            }
            barrier_raw();
        }
        STAMP(8);
        {
            u32 off[4]; u32x4 v[4];
#pragma unroll
            for (int i = 0; i < 4; ++i) {
                const u32 pidx = i * 32 + (lane >> 1);
                off[i] = O_4 + ((pidx < NMLP ? pidx : pidx - 16) * 512 + rb * 256 + (rc >> 1)) * 16;
            }
            sweep<4>(G, off, tag, v);
            float s0 = 0.f, s1 = 0.f;
#pragma unroll
            for (int i = 0; i < 4; ++i) {
                const bool live = (i * 32 + (lane >> 1)) < NMLP;
                s0 += live ? __uint_as_float(v[i][0]) : 0.f;
                s1 += live ? __uint_as_float(v[i][2]) : 0.f;
            }
            s0 += dppf<XOR2>(s0); s0 += dppf<ROR4>(s0); s0 += dppf<ROR8>(s0); s0 = xrow_sum(s0);
            s1 += dppf<XOR2>(s1); s1 += dppf<ROR4>(s1); s1 += dppf<ROR8>(s1); s1 = xrow_sum(s1);
            const u32 a = pk(s0, s1);
            const u32 res = pk(h0 + lo2f(a), h1 + hi2f(a));
            if (lane < 2) *(u32*)(out + rb * 512 + rc) = res;
        }
        STAMP(9);
        if (bid == 0 && lane == 0) __hip_atomic_store((gu32*)(wsg), tag, RLX_AGENT);
    }
}

extern "C" void kernel_launch(void* const* d_in, const int* in_sizes, int n_in,
                              void* d_out, int out_size, void* d_ws, size_t ws_size,
                              hipStream_t stream) {
    mega<<<NBLK, NTHR, 0, stream>>>((const u16*)d_in[0], (const u16*)d_in[1], (const u16*)d_in[2], (const u16*)d_in[3],
                                    (const u16*)d_in[4], (const u16*)d_in[6], (const u16*)d_in[7], (const u16*)d_in[8],
                                    (u16*)d_out, (u32*)d_ws);
}
```

```cpp
#include <hip/hip_runtime.h>
#include <stdint.h>

typedef unsigned short u16;
typedef unsigned int u32;
typedef unsigned long long u64;
typedef __attribute__((ext_vector_type(4))) unsigned u32x4;
typedef __attribute__((ext_vector_type(2))) unsigned u32x2;
typedef __attribute__((ext_vector_type(8))) short s16x8;
typedef __attribute__((ext_vector_type(4))) float f32x4;
typedef __attribute__((ext_vector_type(2))) __bf16 bf16x2;
#define GAS __attribute__((address_space(1)))
#define LAS __attribute__((address_space(3)))
typedef GAS u32 gu32;
#define RLX_AGENT __ATOMIC_RELAXED, __HIP_MEMORY_SCOPE_AGENT

constexpr int SK = 32768, WIN = 4096, FF = 1792;
constexpr int NBLK = 256, NTHR = 1024, NMLP = 112;
constexpr unsigned SPIN_CAP = 1u << 18;

constexpr u32 O_Q = 256;
constexpr u32 O_1O = O_Q + 8 * 2 * 128 * 16;
constexpr u32 O_1ML = O_1O + 128 * 256 * 16;
constexpr u32 O_2 = O_1ML + 128 * 4 * 16;
constexpr u32 O_3 = O_2 + 128 * 512 * 16;
constexpr u32 O_4 = O_3 + 256 * 16;
constexpr u32 O_END = O_4 + NMLP * 512 * 16;

#define STAMP(i) do { } while (0)
#define CSTAMP(i) do { } while (0)

__device__ __forceinline__ float lo2f(u32 v) { return __uint_as_float(v << 16); }
__device__ __forceinline__ float hi2f(u32 v) { return __uint_as_float(v & 0xffff0000u); }
__device__ __forceinline__ u32 pk(float lo, float hi) {
    const bf16x2 v = {(__bf16)lo, (__bf16)hi};
    return __builtin_bit_cast(u32, v);
}
__device__ __forceinline__ float bfr(float f) { return lo2f(pk(f, f)); }
__device__ __forceinline__ u32 packhi(float lo, float hi) {
    return __builtin_amdgcn_perm(__float_as_uint(hi), __float_as_uint(lo), 0x07060302u);
}
__device__ __forceinline__ u32x4 ldg(const u16* p) { return __builtin_nontemporal_load((const u32x4*)p); }
__device__ __forceinline__ u32x4 ldc(const u16* p) { return *(const u32x4*)p; }
__device__ __forceinline__ f32x4 mfma(u32x4 a, u32x4 b, f32x4 c) {
    return __builtin_amdgcn_mfma_f32_16x16x32_bf16(__builtin_bit_cast(s16x8, a), __builtin_bit_cast(s16x8, b), c, 0, 0, 0);
}
__device__ __forceinline__ u32x4 pack2(f32x4 t0, f32x4 t1) {
    u32x4 r;
    r[0] = packhi(t0[0], t0[1]); r[1] = packhi(t0[2], t0[3]);
    r[2] = packhi(t1[0], t1[1]); r[3] = packhi(t1[2], t1[3]);
    return r;
}
template <int CTRL> __device__ __forceinline__ float dppf(float x) {
    return __builtin_bit_cast(float, __builtin_amdgcn_update_dpp(0, __builtin_bit_cast(int, x), CTRL, 0xf, 0xf, true));
}
template <int CTRL> __device__ __forceinline__ u32 dppu(u32 x) {
    return (u32)__builtin_amdgcn_update_dpp(0, (int)x, CTRL, 0xf, 0xf, true);
}
constexpr int XOR1 = 0xB1, XOR2 = 0x4E, HMIR = 0x141, MIR = 0x140, ROR4 = 0x124, ROR8 = 0x128;
__device__ __forceinline__ float x32_sum(float x) {
    auto t = __builtin_amdgcn_permlane32_swap(__float_as_uint(x), __float_as_uint(x), false, false);
    return __uint_as_float(t[0]) + __uint_as_float(t[1]);
}
__device__ __forceinline__ float xrow_sum(float x) {
    auto s = __builtin_amdgcn_permlane16_swap(__float_as_uint(x), __float_as_uint(x), false, false);
    return x32_sum(__uint_as_float(s[0]) + __uint_as_float(s[1]));
}
__device__ __forceinline__ float xrow_max(float x) {
    auto s = __builtin_amdgcn_permlane16_swap(__float_as_uint(x), __float_as_uint(x), false, false);
    x = fmaxf(__uint_as_float(s[0]), __uint_as_float(s[1]));
    auto t = __builtin_amdgcn_permlane32_swap(__float_as_uint(x), __float_as_uint(x), false, false);
    return fmaxf(__uint_as_float(t[0]), __uint_as_float(t[1]));
}
__device__ __forceinline__ float wsum(float v) {
    v += dppf<XOR1>(v); v += dppf<XOR2>(v); v += dppf<HMIR>(v); v += dppf<MIR>(v);
    return xrow_sum(v);
}
__device__ __forceinline__ void barrier_raw() {
    asm volatile("s_waitcnt lgkmcnt(0)" ::: "memory");
    __builtin_amdgcn_s_barrier();
    asm volatile("" ::: "memory");
}
typedef __amdgpu_buffer_rsrc_t rsrc_t;
__device__ __forceinline__ void glds16(const u16* gsrc, u32 lds_dst) {
    u32 keep;
    asm volatile("s_mov_b32 %0, m0\n\ts_mov_b32 m0, %2\n\ts_nop 0\n\tglobal_load_lds_dwordx4 %1, off nt\n\ts_mov_b32 m0, %0"
                 : "=&s"(keep) : "v"(gsrc), "s"(lds_dst) : "memory");
}
__device__ __forceinline__ void put2(rsrc_t rs, u32 off, u32 tag, u32 v0, u32 v1) {
    const u32x4 d = {v0, tag, v1, tag};
    __builtin_amdgcn_raw_buffer_store_b128(d, rs, (int)off, 0, 16  );
}
template <int N>
__device__ __forceinline__ void sweep(rsrc_t rs, const u32 (&off)[N], u32 tag, u32x4 (&v)[N]) {
    for (unsigned spins = 0; spins < SPIN_CAP; ++spins) {
        asm volatile("" ::: "memory");
        bool ok = true;
#pragma unroll
        for (int k = 0; k < N; ++k) v[k] = __builtin_amdgcn_raw_buffer_load_b128(rs, (int)off[k], 0, 16  );
#pragma unroll
        for (int k = 0; k < N; ++k) ok &= (v[k][1] == tag) & (v[k][3] == tag);
        if (__all(ok)) return;
    }
}

__device__ __forceinline__ u32x4 ln_row(u32x4 xr) {
    float v[8];
#pragma unroll
    for (int i = 0; i < 4; ++i) { v[2 * i] = lo2f(xr[i]); v[2 * i + 1] = hi2f(xr[i]); }
    const float s = ((v[0] + v[1]) + (v[2] + v[3])) + ((v[4] + v[5]) + (v[6] + v[7]));
    const float mean = wsum(s) * (1.0f / 512.0f);
    float s2 = 0.f;
#pragma unroll
    for (int i = 0; i < 8; ++i) { const float d = v[i] - mean; s2 += d * d; }
    const float var = wsum(s2) * (1.0f / 512.0f);
    const float mb = bfr(mean);
    const float den = bfr(__builtin_amdgcn_sqrtf(bfr(bfr(var) + 1e-5f)));
    const float inv = __builtin_amdgcn_rcpf(den);
    u32x4 out;
#pragma unroll
    for (int i = 0; i < 4; ++i) {
        const u32 d = pk(v[2 * i] - mb, v[2 * i + 1] - mb);
        out[i] = pk(lo2f(d) * inv, hi2f(d) * inv);
    }
    return out;
}

struct __attribute__((aligned(16))) Lds {
    u16 kbuf[16][32 * 128];
    u16 pl[16][4][4][8];
    float mw[16][4], lw[16][4];
    float part[8][64];
    u16 ln[2][512];
    u16 qs[512];
    u16 os[2][32];
    u16 hh[2][16];
    u32 tag;
};

__global__ void __launch_bounds__(NTHR) mega(const u16* x, const u16* kc, const u16* vc, const u16* wq, const u16* wo,
                                             const u16* wg, const u16* wu, const u16* wd, u16* out, u32* wsg) {
    __shared__ Lds L;
    const int tid = threadIdx.x, lane = tid & 63;
    const int w = __builtin_amdgcn_readfirstlane(tid >> 6);
    const int bid = blockIdx.x;
    const int kvh = bid & 7, idx = bid >> 3;
    const bool is_attn = idx < 16;
    const int b = (idx >> 3) & 1, s = idx & 7;
    const int ll = idx - 16;
    const bool is_mlp = ll >= 0 && ll < 14;
    const int mblk = kvh * 14 + ll, mc0 = mblk * 16;
    const rsrc_t G = __builtin_amdgcn_make_buffer_rsrc((void*)wsg, 0, (int)O_END, 0x00020000);
    const int g = lane >> 4, li = lane & 15;
    const f32x4 zero4 = {0.f, 0.f, 0.f, 0.f};
    u32 mk[4];
#pragma unroll
    for (int d = 0; d < 4; ++d) mk[d] = (((li & 7) >> 1) == d) ? 0xFFFFFFFFu : 0u;
    const u32 sh = 16 * (li & 1);

    const bool is_comm = (w == 15);
    const int rb = bid >> 7, rc = (bid & 127) * 4 + (lane & 1) * 2;
    u32 tagc = 0, xpair = 0;
    if (is_comm) {
        STAMP(0);
        tagc = __hip_atomic_load((gu32*)(wsg), RLX_AGENT) + 1u;
        xpair = *(const u32*)(x + rb * 512 + rc);
        if (lane == 0) L.tag = tagc;
    }
    if (is_attn) {
        u32x4 vr[8];
        const size_t kvoff = ((size_t)(b * 8 + kvh) * SK + (SK - WIN) + s * 512 + w * 32) * 128;
        {
            const u32 kdst = (u32)(uintptr_t)(LAS u16*)&L.kbuf[w][0];
#pragma unroll
            for (int j = 0; j < 8; ++j)
                glds16(kc + kvoff + (size_t)(4 * j + g) * 128 + ((li ^ (4 * (j & 3) + g)) * 8), kdst + j * 1024);
        }
        if (is_comm) {
            u32 off[2]; u32x4 v[2];
#pragma unroll
            for (int k = 0; k < 2; ++k) off[k] = O_Q + ((kvh * 2 + b) * 128 + k * 64 + lane) * 16;
            sweep<2>(G, off, tagc, v);
#pragma unroll
            for (int k = 0; k < 2; ++k) *(u32x2*)&L.qs[(k * 64 + lane) * 4] = u32x2{v[k][0], v[k][2]};
            STAMP(2);
            barrier_raw();
        }
        CSTAMP(12);
        if (!is_comm) barrier_raw();
        CSTAMP(13);
#pragma unroll
        for (int j = 0; j < 8; ++j) vr[j] = ldg(vc + kvoff + (size_t)j * 512 + lane * 8);
        asm volatile("" ::: "memory");
        float* ow = (float*)&L.kbuf[w][0];
        {
            asm volatile("s_waitcnt vmcnt(8)" ::: "memory");
            f32x4 st[2] = {zero4, zero4};
#pragma unroll
            for (int kk = 0; kk < 4; ++kk) {
                const u32x4 qf = *(const u32x4*)&L.qs[(li & 3) * 128 + kk * 32 + g * 8];
#pragma unroll
                for (int t = 0; t < 2; ++t) {
                    const u32x4 kf = *(const u32x4*)&L.kbuf[w][(t * 16 + li) * 128 + (((kk * 4 + g) ^ li) * 8)];
                    st[t] = mfma(kf, qf, st[t]);
                }
            }
            CSTAMP(14);
            float sc[8];
#pragma unroll
            for (int t = 0; t < 2; ++t)
#pragma unroll
                for (int i = 0; i < 2; ++i) {
                    const u32 a = pk(st[t][2 * i], st[t][2 * i + 1]);
                    const u32 c = pk(lo2f(a) * 0.08838834764831845f, hi2f(a) * 0.08838834764831845f);
                    sc[t * 4 + 2 * i] = lo2f(c); sc[t * 4 + 2 * i + 1] = hi2f(c);
                }
            float m = fmaxf(fmaxf(fmaxf(sc[0], sc[1]), fmaxf(sc[2], sc[3])), fmaxf(fmaxf(sc[4], sc[5]), fmaxf(sc[6], sc[7])));
            m = xrow_max(m);
            float p[8], l = 0.f;
#pragma unroll
            for (int i = 0; i < 8; ++i) { p[i] = __expf(sc[i] - m); l += p[i]; }
            l = xrow_sum(l);
#pragma unroll
            for (int t = 0; t < 2; ++t)
#pragma unroll
                for (int i = 0; i < 2; ++i) {
                    const u32 pp = pk(p[t * 4 + 2 * i], p[t * 4 + 2 * i + 1]);
                    L.pl[w][li & 3][2 * i][t * 4 + g] = (u16)(pp & 0xffffu);
                    L.pl[w][li & 3][2 * i + 1][t * 4 + g] = (u16)(pp >> 16);
                }
            if (lane < 4) { L.mw[w][lane] = m; L.lw[w][lane] = l; }
            asm volatile("s_waitcnt lgkmcnt(0)" ::: "memory");
            CSTAMP(15);
            u32x4 pv[2];
#pragma unroll
            for (int hp = 0; hp < 2; ++hp) pv[hp] = *(const u32x4*)&L.pl[w][2 * hp + (li >> 3)][g][0];
            f32x4 oacc[2] = {zero4, zero4};
#pragma unroll
            for (int j = 0; j < 8; ++j)
#pragma unroll
                for (int hp = 0; hp < 2; ++hp) {
                    const u32 xv = (j & 1) ? (pv[hp][j >> 1] >> 16) : (pv[hp][j >> 1] & 0xffffu);
                    const u32 val = xv << sh;
                    const u32x4 bf = {val & mk[0], val & mk[1], val & mk[2], val & mk[3]};
                    oacc[hp] = mfma(vr[j], bf, oacc[hp]);
                }
#pragma unroll
            for (int hp = 0; hp < 2; ++hp)
#pragma unroll
                for (int ii = 0; ii < 4; ++ii) ow[(2 * hp + (li >> 3)) * 128 + (g * 4 + ii) * 8 + (li & 7)] = oacc[hp][ii];
        }
        CSTAMP(16);
        barrier_raw();
        CSTAMP(17);
        if (w < 8) {
            const u32 tag = L.tag;
            const int head = w >> 1, e0 = (w & 1) * 64 + (lane & 31) * 2, jh = lane >> 5;
            float M = L.mw[0][head];
#pragma unroll
            for (int j = 1; j < 16; ++j) M = fmaxf(M, L.mw[j][head]);
            float o0 = 0.f, o1 = 0.f, ls = 0.f;
#pragma unroll
            for (int j = 0; j < 8; ++j) {
                const int jw = jh * 8 + j;
                const float f = __expf(L.mw[jw][head] - M);
                const float2 ov = *(const float2*)((const float*)&L.kbuf[jw][0] + head * 128 + e0);
                o0 += f * ov.x; o1 += f * ov.y;
                ls += f * L.lw[jw][head];
            }
            o0 = x32_sum(o0); o1 = x32_sum(o1); ls = x32_sum(ls);
            const u32 p1 = (kvh * 2 + b) * 8 + s;
            if (lane < 32) put2(G, O_1O + (p1 * 256 + head * 64 + (w & 1) * 32 + lane) * 16, tag, __float_as_uint(o0), __float_as_uint(o1));
            if (lane == 32 && (w & 1) == 0) put2(G, O_1ML + (p1 * 4 + head) * 16, tag, __float_as_uint(M), __float_as_uint(ls));
        }
        CSTAMP(18);
        if (!is_comm) return;
        STAMP(3);
        __builtin_amdgcn_s_sleep(64);
    } else if (!is_comm) {
        if (w >= 8) return;
        u32x4 wqr[4], wor[4], gur[4], wdr[2];
        __builtin_amdgcn_s_barrier();
        asm volatile("" ::: "memory");
#pragma unroll
        for (int t = 0; t < 4; ++t) wqr[t] = ldc(wq + (size_t)(w * 64 + t * 16 + li) * 4096 + kvh * 512 + ll * 32 + g * 8);
        asm volatile("" ::: "memory");
        u32x4 sel[2];
#pragma unroll
        for (int h = 0; h < 2; ++h) {
            const bool on = (g == 2 * h + (li >> 3));
            const u32 one = ((li & 1) ? 0x3F800000u : 0x00003F80u);
#pragma unroll
            for (int i = 0; i < 4; ++i) sel[h][i] = (on && ((li & 7) >> 1) == i) ? one : 0u;
        }
        barrier_raw();
        {
            f32x4 acc[2] = {zero4, zero4};
#pragma unroll
            for (int pp = 0; pp < 2; ++pp) {
                const u32x2 a0 = *(const u32x2*)&L.ln[li & 1][w * 64 + pp * 32 + g * 4];
                const u32x2 a1 = *(const u32x2*)&L.ln[li & 1][w * 64 + pp * 32 + 16 + g * 4];
                const u32x4 bf = {a0[0], a0[1], a1[0], a1[1]};
#pragma unroll
                for (int h = 0; h < 2; ++h)
                    acc[h] = mfma(pack2(mfma(wqr[2 * pp], sel[h], zero4), mfma(wqr[2 * pp + 1], sel[h], zero4)), bf, acc[h]);
            }
            if (li < 2) {
                *(f32x4*)&L.part[w][li * 32 + g * 4] = acc[0];
                *(f32x4*)&L.part[w][li * 32 + 16 + g * 4] = acc[1];
            }
        }
        barrier_raw();
        __builtin_amdgcn_s_barrier();
        asm volatile("" ::: "memory");
#pragma unroll
        for (int j = 0; j < 4; ++j)
            wor[j] = ldg(wo + (size_t)(kvh * 512 + ll * 32 + 8 * j + (lane >> 3)) * 512 + w * 64 + (lane & 7) * 8);
        {
            const u16* gu = is_mlp ? ((g < 2) ? wg : wu) + (size_t)(w * 64 + li) * FF + mc0 + (g & 1) * 8 : wo;
            const size_t gstep = is_mlp ? (size_t)16 * FF : 0;
#pragma unroll
            for (int t = 0; t < 4; ++t) gur[t] = ldc(gu + t * gstep);
            const u16* dp = is_mlp ? wd + (size_t)(mc0 + (lane >> 3)) * 512 + w * 64 + (lane & 7) * 8 : wo;
#pragma unroll
            for (int j = 0; j < 2; ++j) wdr[j] = ldg(dp + (is_mlp ? j * 8 * 512 : 0));
        }
        asm volatile("" ::: "memory");
        u32x4 guT[4];
#pragma unroll
        for (int pp = 0; pp < 2; ++pp)
#pragma unroll
            for (int mt = 0; mt < 2; ++mt)
                guT[pp * 2 + mt] = pack2(mfma(gur[2 * pp], sel[mt], zero4), mfma(gur[2 * pp + 1], sel[mt], zero4));
        asm volatile("" : "+v"(guT[0]), "+v"(guT[1]), "+v"(guT[2]), "+v"(guT[3]), "+v"(wor[0]), "+v"(wor[1]), "+v"(wor[2]), "+v"(wor[3]), "+v"(wdr[0]), "+v"(wdr[1]));
        barrier_raw();
        CSTAMP(19);
        const u32 tag = L.tag;
        const bool dvalid = (li >> 3) == (g >> 1);
        const int rep = (g >> 1) * 2 + (li >> 3);
#pragma unroll
        for (int bb = 0; bb < 2; ++bb) {
            f32x4 d = zero4;
#pragma unroll
            for (int j = 0; j < 4; ++j) {
                const u32 pr = ((const u32*)L.os[bb])[4 * j + g];
                const u32 xv = (li >> 3) ? (pr >> 16) : (pr & 0xffffu);
                const u32 val = xv << sh;
                const u32x4 bf = {val & mk[0], val & mk[1], val & mk[2], val & mk[3]};
                d = mfma(wor[j], bf, d);
            }
            float y[4];
#pragma unroll
            for (int ii = 0; ii < 4; ++ii) {
                float v = dvalid ? d[ii] : 0.f;
                v += dppf<ROR8>(v);
                y[ii] = x32_sum(v);
            }
            const float v0 = (rep & 2) ? ((rep & 1) ? y[3] : y[2]) : ((rep & 1) ? y[1] : y[0]);
            const float v1 = dppf<XOR1>(v0);
            if ((li & 1) == 0)
                put2(G, O_2 + (((kvh * 16 + ll) * 2 + bb) * 256 + w * 32 + ((g & 1) * 4 + rep) * 4 + ((li & 7) >> 1)) * 16, tag,
                     __float_as_uint(v0), __float_as_uint(v1));
        }
        CSTAMP(20);
        if (is_mlp) {
            barrier_raw();
            CSTAMP(21);
            f32x4 ag = zero4, au = zero4;
#pragma unroll
            for (int pp = 0; pp < 2; ++pp) {
                const u32x2 a0 = *(const u32x2*)&L.ln[li & 1][w * 64 + pp * 32 + g * 4];
                const u32x2 a1 = *(const u32x2*)&L.ln[li & 1][w * 64 + pp * 32 + 16 + g * 4];
                const u32x4 bf = {a0[0], a0[1], a1[0], a1[1]};
                ag = mfma(guT[pp * 2], bf, ag);
                au = mfma(guT[pp * 2 + 1], bf, au);
            }
            if (li < 2) {
                *(f32x4*)&L.part[w][li * 16 + g * 4] = ag;
                *(f32x4*)&L.part[w][32 + li * 16 + g * 4] = au;
            }
            CSTAMP(23);
            barrier_raw();
            barrier_raw();
            CSTAMP(24);
#pragma unroll
            for (int bb = 0; bb < 2; ++bb) {
                f32x4 d = zero4;
#pragma unroll
                for (int j = 0; j < 2; ++j) {
                    const u32 pr = ((const u32*)L.hh[bb])[4 * j + g];
                    const u32 xv = (li >> 3) ? (pr >> 16) : (pr & 0xffffu);
                    const u32 val = xv << sh;
                    const u32x4 bf = {val & mk[0], val & mk[1], val & mk[2], val & mk[3]};
                    d = mfma(wdr[j], bf, d);
                }
                float y[4];
#pragma unroll
                for (int ii = 0; ii < 4; ++ii) {
                    float v = dvalid ? d[ii] : 0.f;
                    v += dppf<ROR8>(v);
                    y[ii] = x32_sum(v);
                }
                const float v0 = (rep & 2) ? ((rep & 1) ? y[3] : y[2]) : ((rep & 1) ? y[1] : y[0]);
                const float v1 = dppf<XOR1>(v0);
                if ((li & 1) == 0)
                    put2(G, O_4 + (mblk * 512 + bb * 256 + w * 32 + ((g & 1) * 4 + rep) * 4 + ((li & 7) >> 1)) * 16, tag,
                         __float_as_uint(v0), __float_as_uint(v1));
            }
            CSTAMP(25);
        }
        return;
    }
    {
        const u32 tag = tagc;
        if (!is_attn) {
            u32x4 xr[2];
#pragma unroll
            for (int bb = 0; bb < 2; ++bb) xr[bb] = ldc(x + bb * 512 + lane * 8);
            asm volatile("" ::: "memory");
            __builtin_amdgcn_s_barrier();
            asm volatile("" ::: "memory");
            *(u32x4*)&L.ln[0][lane * 8] = ln_row(xr[0]);
            *(u32x4*)&L.ln[1][lane * 8] = ln_row(xr[1]);
            STAMP(1);
            barrier_raw();
            barrier_raw();
            {
                float q = 0.f;
#pragma unroll
                for (int j = 0; j < 8; ++j) q += L.part[j][lane];
                const u32 pr = pk(q, dppf<XOR1>(q));
                const u32 pr2 = dppu<XOR2>(pr);
                STAMP(26);
                if ((lane & 3) == 0) put2(G, O_Q + ((kvh * 2 + (lane >> 5)) * 128 + ll * 8 + ((lane & 31) >> 2)) * 16, tag, pr, pr2);
                asm volatile("" ::: "memory");
                __builtin_amdgcn_s_barrier();
                __builtin_amdgcn_s_sleep(100);
            }
            {
                const u32 head = ll >> 2;
                u32 off[5]; u32x4 v[5];
#pragma unroll
                for (int i = 0; i < 4; ++i)
                    off[i] = O_1O + ((((kvh * 2 + (i >> 1)) * 8 + (i & 1) * 4 + g)) * 256 + ll * 16 + li) * 16;
                off[4] = O_1ML + (((kvh * 2 + (li >> 3)) * 8 + (li & 7)) * 4 + head) * 16;
                sweep<5>(G, off, tag, v);
                const float mj = __uint_as_float(v[4][0]), lj = __uint_as_float(v[4][2]);
                float M = fmaxf(mj, dppf<XOR1>(mj)); M = fmaxf(M, dppf<XOR2>(M)); M = fmaxf(M, dppf<HMIR>(M));
                const float wj = __expf(mj - M);
                float ls = wj * lj;
                ls += dppf<XOR1>(ls); ls += dppf<XOR2>(ls); ls += dppf<HMIR>(ls);
                const float wn = wj * __builtin_amdgcn_rcpf(ls);
                float o[2][2] = {{0.f, 0.f}, {0.f, 0.f}};
#pragma unroll
                for (int i = 0; i < 4; ++i) {
                    const int base = (i >> 1) * 8 + (i & 1) * 4;
                    const float w0 = __builtin_bit_cast(float, __builtin_amdgcn_readlane(__builtin_bit_cast(int, wn), base));
                    const float w1 = __builtin_bit_cast(float, __builtin_amdgcn_readlane(__builtin_bit_cast(int, wn), base + 1));
                    const float w2 = __builtin_bit_cast(float, __builtin_amdgcn_readlane(__builtin_bit_cast(int, wn), base + 2));
                    const float w3 = __builtin_bit_cast(float, __builtin_amdgcn_readlane(__builtin_bit_cast(int, wn), base + 3));
                    const float ww = (g & 2) ? ((g & 1) ? w3 : w2) : ((g & 1) ? w1 : w0);
                    o[i >> 1][0] += ww * __uint_as_float(v[i][0]);
                    o[i >> 1][1] += ww * __uint_as_float(v[i][2]);
                }
#pragma unroll
                for (int bb = 0; bb < 2; ++bb) {
                    const float a0 = xrow_sum(o[bb][0]), a1 = xrow_sum(o[bb][1]);
                    if (lane < 16) ((u32*)L.os[bb])[lane] = pk(a0, a1);
                }
            }
            STAMP(4);
            barrier_raw();
        }
        float h0, h1;
        {
            u32 off[4]; u32x4 v[4];
#pragma unroll
            for (int i = 0; i < 4; ++i) off[i] = O_2 + (((i * 32 + (lane >> 1)) * 2 + rb) * 256 + (rc >> 1)) * 16;
            sweep<4>(G, off, tag, v);
            float s0 = 0.f, s1 = 0.f;
#pragma unroll
            for (int i = 0; i < 4; ++i) { s0 += __uint_as_float(v[i][0]); s1 += __uint_as_float(v[i][2]); }
            s0 += dppf<XOR2>(s0); s0 += dppf<ROR4>(s0); s0 += dppf<ROR8>(s0); s0 = xrow_sum(s0);
            s1 += dppf<XOR2>(s1); s1 += dppf<ROR4>(s1); s1 += dppf<ROR8>(s1); s1 = xrow_sum(s1);
            const u32 a = pk(s0, s1);
            const u32 pr = pk(lo2f(xpair) + lo2f(a), hi2f(xpair) + hi2f(a));
            h0 = lo2f(pr); h1 = hi2f(pr);
            const u32 pr2 = dppu<XOR1>(pr);
            if (lane == 0) put2(G, O_3 + bid * 16, tag, pr, pr2);
        }
        STAMP(5);
        if (is_mlp) {
            u32 off[4]; u32x4 v[4];
#pragma unroll
            for (int i = 0; i < 4; ++i) off[i] = O_3 + (i * 64 + lane) * 16;
            sweep<4>(G, off, tag, v);
            STAMP(6);
#pragma unroll
            for (int bb = 0; bb < 2; ++bb) {
                const u32x4 hr = {v[2 * bb][0], v[2 * bb][2], v[2 * bb + 1][0], v[2 * bb + 1][2]};
                const u32x4 ln = ln_row(hr);
                *(u32x2*)&L.ln[bb][lane * 4] = u32x2{ln[0], ln[1]};
                *(u32x2*)&L.ln[bb][256 + lane * 4] = u32x2{ln[2], ln[3]};
            }
            barrier_raw();
            barrier_raw();
            STAMP(7);
            {
                float a = 0.f;
#pragma unroll
                for (int j = 0; j < 8; ++j) a += L.part[j][lane];
                a = bfr(a);
                auto t = __builtin_amdgcn_permlane32_swap(__float_as_uint(a), __float_as_uint(a), false, false);
                const float u = __uint_as_float(t[1]);
                const float sg = bfr(__builtin_amdgcn_rcpf(1.0f + __expf(-a)));
                const float hv = bfr(sg * u);
                if (lane < 32) L.hh[lane >> 4][lane & 15] = (u16)(__float_as_uint(hv) >> 16);
            }
            barrier_raw();
        }
        else __builtin_amdgcn_s_sleep(90);
        STAMP(8);
        {
            u32 off[4]; u32x4 v[4];
#pragma unroll
            for (int i = 0; i < 4; ++i) {
                const u32 pidx = i * 32 + (lane >> 1);
                off[i] = O_4 + ((pidx < NMLP ? pidx : pidx - 16) * 512 + rb * 256 + (rc >> 1)) * 16;
            }
            sweep<4>(G, off, tag, v);
            float s0 = 0.f, s1 = 0.f;
#pragma unroll
            for (int i = 0; i < 4; ++i) {
                const bool live = (i * 32 + (lane >> 1)) < NMLP;
                s0 += live ? __uint_as_float(v[i][0]) : 0.f;
                s1 += live ? __uint_as_float(v[i][2]) : 0.f;
            }
            s0 += dppf<XOR2>(s0); s0 += dppf<ROR4>(s0); s0 += dppf<ROR8>(s0); s0 = xrow_sum(s0);
            s1 += dppf<XOR2>(s1); s1 += dppf<ROR4>(s1); s1 += dppf<ROR8>(s1); s1 = xrow_sum(s1);
            const u32 a = pk(s0, s1);
            const u32 res = pk(h0 + lo2f(a), h1 + hi2f(a));
            if (lane < 2) *(u32*)(out + rb * 512 + rc) = res;
        }
        STAMP(9);
        if (bid == 0 && lane == 0) __hip_atomic_store((gu32*)(wsg), tag, RLX_AGENT);
    }
}

extern "C" void kernel_launch(void* const* d_in, const int* in_sizes, int n_in,
                              void* d_out, int out_size, void* d_ws, size_t ws_size,
                              hipStream_t stream) {
    mega<<<NBLK, NTHR, 0, stream>>>((const u16*)d_in[0], (const u16*)d_in[1], (const u16*)d_in[2], (const u16*)d_in[3],
                                    (const u16*)d_in[4], (const u16*)d_in[6], (const u16*)d_in[7], (const u16*)d_in[8],
                                    (u16*)d_out, (u32*)d_ws);
}
```

```cpp
#include <hip/hip_runtime.h>
#include <stdint.h>

typedef unsigned short u16;
typedef unsigned int u32;
typedef unsigned long long u64;
typedef __attribute__((ext_vector_type(4))) unsigned u32x4;
typedef __attribute__((ext_vector_type(2))) unsigned u32x2;
typedef __attribute__((ext_vector_type(8))) short s16x8;
typedef __attribute__((ext_vector_type(4))) float f32x4;
typedef __attribute__((ext_vector_type(2))) __bf16 bf16x2;
#define GAS __attribute__((address_space(1)))
#define LAS __attribute__((address_space(3)))
typedef GAS u32 gu32;
#define RLX_AGENT __ATOMIC_RELAXED, __HIP_MEMORY_SCOPE_AGENT

constexpr int SK = 32768, WIN = 4096, FF = 1792;
constexpr int NBLK = 256, NTHR = 1024, NMLP = 112;
constexpr unsigned SPIN_CAP = 1u << 18;

constexpr u32 O_Q = 256;
constexpr u32 O_1O = O_Q + 8 * 2 * 128 * 16;
constexpr u32 O_1ML = O_1O + 128 * 256 * 16;
constexpr u32 O_2 = O_1ML + 128 * 4 * 16;
constexpr u32 O_3 = O_2 + 128 * 512 * 16;
constexpr u32 O_4 = O_3 + 256 * 16;
constexpr u32 O_END = O_4 + 256 * 256 * 16;

#define STAMP(i) do { } while (0)
#define CSTAMP(i) do { } while (0)

__device__ __forceinline__ float lo2f(u32 v) { return __uint_as_float(v << 16); }
__device__ __forceinline__ float hi2f(u32 v) { return __uint_as_float(v & 0xffff0000u); }
__device__ __forceinline__ u32 pk(float lo, float hi) {
    const bf16x2 v = {(__bf16)lo, (__bf16)hi};
    return __builtin_bit_cast(u32, v);
}
__device__ __forceinline__ float bfr(float f) { return lo2f(pk(f, f)); }
__device__ __forceinline__ u32 packhi(float lo, float hi) {
    return __builtin_amdgcn_perm(__float_as_uint(hi), __float_as_uint(lo), 0x07060302u);
}
__device__ __forceinline__ u32x4 ldg(const u16* p) { return __builtin_nontemporal_load((const u32x4*)p); }
__device__ __forceinline__ u32x4 ldc(const u16* p) { return *(const u32x4*)p; }
__device__ __forceinline__ f32x4 mfma(u32x4 a, u32x4 b, f32x4 c) {
    return __builtin_amdgcn_mfma_f32_16x16x32_bf16(__builtin_bit_cast(s16x8, a), __builtin_bit_cast(s16x8, b), c, 0, 0, 0);
}
__device__ __forceinline__ u32x4 pack2(f32x4 t0, f32x4 t1) {
    u32x4 r;
    r[0] = packhi(t0[0], t0[1]); r[1] = packhi(t0[2], t0[3]);
    r[2] = packhi(t1[0], t1[1]); r[3] = packhi(t1[2], t1[3]);
    return r;
}
template <int CTRL> __device__ __forceinline__ float dppf(float x) {
    return __builtin_bit_cast(float, __builtin_amdgcn_update_dpp(0, __builtin_bit_cast(int, x), CTRL, 0xf, 0xf, true));
}
template <int CTRL> __device__ __forceinline__ u32 dppu(u32 x) {
    return (u32)__builtin_amdgcn_update_dpp(0, (int)x, CTRL, 0xf, 0xf, true);
}
constexpr int XOR1 = 0xB1, XOR2 = 0x4E, HMIR = 0x141, MIR = 0x140, ROR4 = 0x124, ROR8 = 0x128;
__device__ __forceinline__ float x32_sum(float x) {
    auto t = __builtin_amdgcn_permlane32_swap(__float_as_uint(x), __float_as_uint(x), false, false);
    return __uint_as_float(t[0]) + __uint_as_float(t[1]);
}
__device__ __forceinline__ float xrow_sum(float x) {
    auto s = __builtin_amdgcn_permlane16_swap(__float_as_uint(x), __float_as_uint(x), false, false);
    return x32_sum(__uint_as_float(s[0]) + __uint_as_float(s[1]));
}
__device__ __forceinline__ float xrow_max(float x) {
    auto s = __builtin_amdgcn_permlane16_swap(__float_as_uint(x), __float_as_uint(x), false, false);
    x = fmaxf(__uint_as_float(s[0]), __uint_as_float(s[1]));
    auto t = __builtin_amdgcn_permlane32_swap(__float_as_uint(x), __float_as_uint(x), false, false);
    return fmaxf(__uint_as_float(t[0]), __uint_as_float(t[1]));
}
__device__ __forceinline__ float wsum(float v) {
    v += dppf<XOR1>(v); v += dppf<XOR2>(v); v += dppf<HMIR>(v); v += dppf<MIR>(v);
    return xrow_sum(v);
}
__device__ __forceinline__ void barrier_raw() {
    asm volatile("s_waitcnt lgkmcnt(0)" ::: "memory");
    __builtin_amdgcn_s_barrier();
    asm volatile("" ::: "memory");
}
typedef __amdgpu_buffer_rsrc_t rsrc_t;
__device__ __forceinline__ void glds16(const u16* gsrc, u32 lds_dst) {
    u32 keep;
    asm volatile("s_mov_b32 %0, m0\n\ts_mov_b32 m0, %2\n\ts_nop 0\n\tglobal_load_lds_dwordx4 %1, off nt\n\ts_mov_b32 m0, %0"
                 : "=&s"(keep) : "v"(gsrc), "s"(lds_dst) : "memory");
}
__device__ __forceinline__ void put2(rsrc_t rs, u32 off, u32 tag, u32 v0, u32 v1) {
    const u32x4 d = {v0, tag, v1, tag};
    __builtin_amdgcn_raw_buffer_store_b128(d, rs, (int)off, 0, 16  );
}
template <int N>
__device__ __forceinline__ void sweep(rsrc_t rs, const u32 (&off)[N], u32 tag, u32x4 (&v)[N]) {
    for (unsigned spins = 0; spins < SPIN_CAP; ++spins) {
        asm volatile("" ::: "memory");
        bool ok = true;
#pragma unroll
        for (int k = 0; k < N; ++k) v[k] = __builtin_amdgcn_raw_buffer_load_b128(rs, (int)off[k], 0, 16  );
#pragma unroll
        for (int k = 0; k < N; ++k) ok &= (v[k][1] == tag) & (v[k][3] == tag);
        if (__all(ok)) return;
    }
}

__device__ __forceinline__ u32x4 ln_row(u32x4 xr) {
    float v[8];
#pragma unroll
    for (int i = 0; i < 4; ++i) { v[2 * i] = lo2f(xr[i]); v[2 * i + 1] = hi2f(xr[i]); }
    const float s = ((v[0] + v[1]) + (v[2] + v[3])) + ((v[4] + v[5]) + (v[6] + v[7]));
    const float mean = wsum(s) * (1.0f / 512.0f);
    float s2 = 0.f;
#pragma unroll
    for (int i = 0; i < 8; ++i) { const float d = v[i] - mean; s2 += d * d; }
    const float var = wsum(s2) * (1.0f / 512.0f);
    const float mb = bfr(mean);
    const float den = bfr(__builtin_amdgcn_sqrtf(bfr(bfr(var) + 1e-5f)));
    const float inv = __builtin_amdgcn_rcpf(den);
    u32x4 out;
#pragma unroll
    for (int i = 0; i < 4; ++i) {
        const u32 d = pk(v[2 * i] - mb, v[2 * i + 1] - mb);
        out[i] = pk(lo2f(d) * inv, hi2f(d) * inv);
    }
    return out;
}

struct __attribute__((aligned(16))) Lds {
    u16 kbuf[16][32 * 128];
    u16 pl[16][4][4][8];
    float mw[16][4], lw[16][4];
    float part[8][64];
    u16 ln[2][512];
    u16 qs[512];
    u16 os[2][32];
    u16 hh[2][16];
    u32 tag;
};

__global__ void __launch_bounds__(NTHR) mega(const u16* x, const u16* kc, const u16* vc, const u16* wq, const u16* wo,
                                             const u16* wg, const u16* wu, const u16* wd, u16* out, u32* wsg) {
    __shared__ Lds L;
    const int tid = threadIdx.x, lane = tid & 63;
    const int w = __builtin_amdgcn_readfirstlane(tid >> 6);
    const int bid = blockIdx.x;
    const int kvh = bid & 7, idx = bid >> 3;
    const bool is_attn = idx < 16;
    const int b = (idx >> 3) & 1, s = idx & 7;
    const int ll = idx - 16;
    const bool is_mlp = ll >= 0 && ll < 14;
    const int mblk = kvh * 14 + ll, mc0 = mblk * 16;
    const rsrc_t G = __builtin_amdgcn_make_buffer_rsrc((void*)wsg, 0, (int)O_END, 0x00020000);
    const int g = lane >> 4, li = lane & 15;
    const f32x4 zero4 = {0.f, 0.f, 0.f, 0.f};
    u32 mk[4];
#pragma unroll
    for (int d = 0; d < 4; ++d) mk[d] = (((li & 7) >> 1) == d) ? 0xFFFFFFFFu : 0u;
    const u32 sh = 16 * (li & 1);

    const bool is_comm = (w == 15);
    const int rb = bid >> 7, rc = (bid & 127) * 4 + (lane & 1) * 2;
    u32 tagc = 0, xpair = 0;
    if (is_comm) {
        STAMP(0);
        tagc = __hip_atomic_load((gu32*)(wsg), RLX_AGENT) + 1u;
        xpair = *(const u32*)(x + rb * 512 + rc);
        if (lane == 0) L.tag = tagc;
    }
    if (is_attn) {
        u32x4 vr[8];
        const size_t kvoff = ((size_t)(b * 8 + kvh) * SK + (SK - WIN) + s * 512 + w * 32) * 128;
        {
            const u32 kdst = (u32)(uintptr_t)(LAS u16*)&L.kbuf[w][0];
#pragma unroll
            for (int j = 0; j < 8; ++j)
                glds16(kc + kvoff + (size_t)(4 * j + g) * 128 + ((li ^ (4 * (j & 3) + g)) * 8), kdst + j * 1024);
        }
        if (is_comm) {
            u32 off[2]; u32x4 v[2];
#pragma unroll
            for (int k = 0; k < 2; ++k) off[k] = O_Q + ((kvh * 2 + b) * 128 + k * 64 + lane) * 16;
            sweep<2>(G, off, tagc, v);
#pragma unroll
            for (int k = 0; k < 2; ++k) *(u32x2*)&L.qs[(k * 64 + lane) * 4] = u32x2{v[k][0], v[k][2]};
            STAMP(2);
            barrier_raw();
        }
        CSTAMP(12);
        if (!is_comm) barrier_raw();
        CSTAMP(13);
#pragma unroll
        for (int j = 0; j < 8; ++j) vr[j] = ldg(vc + kvoff + (size_t)j * 512 + lane * 8);
        asm volatile("" ::: "memory");
        float* ow = (float*)&L.kbuf[w][0];
        {
            asm volatile("s_waitcnt vmcnt(8)" ::: "memory");
            f32x4 st[2] = {zero4, zero4};
#pragma unroll
            for (int kk = 0; kk < 4; ++kk) {
                const u32x4 qf = *(const u32x4*)&L.qs[(li & 3) * 128 + kk * 32 + g * 8];
#pragma unroll
                for (int t = 0; t < 2; ++t) {
                    const u32x4 kf = *(const u32x4*)&L.kbuf[w][(t * 16 + li) * 128 + (((kk * 4 + g) ^ li) * 8)];
                    st[t] = mfma(kf, qf, st[t]);
                }
            }
            CSTAMP(14);
            float sc[8];
#pragma unroll
            for (int t = 0; t < 2; ++t)
#pragma unroll
                for (int i = 0; i < 2; ++i) {
                    const u32 a = pk(st[t][2 * i], st[t][2 * i + 1]);
                    const u32 c = pk(lo2f(a) * 0.08838834764831845f, hi2f(a) * 0.08838834764831845f);
                    sc[t * 4 + 2 * i] = lo2f(c); sc[t * 4 + 2 * i + 1] = hi2f(c);
                }
            float m = fmaxf(fmaxf(fmaxf(sc[0], sc[1]), fmaxf(sc[2], sc[3])), fmaxf(fmaxf(sc[4], sc[5]), fmaxf(sc[6], sc[7])));
            m = xrow_max(m);
            float p[8], l = 0.f;
#pragma unroll
            for (int i = 0; i < 8; ++i) { p[i] = __expf(sc[i] - m); l += p[i]; }
            l = xrow_sum(l);
#pragma unroll
            for (int t = 0; t < 2; ++t)
#pragma unroll
                for (int i = 0; i < 2; ++i) {
                    const u32 pp = pk(p[t * 4 + 2 * i], p[t * 4 + 2 * i + 1]);
                    L.pl[w][li & 3][2 * i][t * 4 + g] = (u16)(pp & 0xffffu);
                    L.pl[w][li & 3][2 * i + 1][t * 4 + g] = (u16)(pp >> 16);
                }
            if (lane < 4) { L.mw[w][lane] = m; L.lw[w][lane] = l; }
            asm volatile("s_waitcnt lgkmcnt(0)" ::: "memory");
            CSTAMP(15);
            u32x4 pv[2];
#pragma unroll
            for (int hp = 0; hp < 2; ++hp) pv[hp] = *(const u32x4*)&L.pl[w][2 * hp + (li >> 3)][g][0];
            f32x4 oacc[2] = {zero4, zero4};
#pragma unroll
            for (int j = 0; j < 8; ++j)
#pragma unroll
                for (int hp = 0; hp < 2; ++hp) {
                    const u32 xv = (j & 1) ? (pv[hp][j >> 1] >> 16) : (pv[hp][j >> 1] & 0xffffu);
                    const u32 val = xv << sh;
                    const u32x4 bf = {val & mk[0], val & mk[1], val & mk[2], val & mk[3]};
                    oacc[hp] = mfma(vr[j], bf, oacc[hp]);
                }
#pragma unroll
            for (int hp = 0; hp < 2; ++hp)
#pragma unroll
                for (int ii = 0; ii < 4; ++ii) ow[(2 * hp + (li >> 3)) * 128 + (g * 4 + ii) * 8 + (li & 7)] = oacc[hp][ii];
        }
        CSTAMP(16);
        barrier_raw();
        CSTAMP(17);
        if (w < 8) {
            const u32 tag = L.tag;
            const int head = w >> 1, e0 = (w & 1) * 64 + (lane & 31) * 2, jh = lane >> 5;
            float M = L.mw[0][head];
#pragma unroll
            for (int j = 1; j < 16; ++j) M = fmaxf(M, L.mw[j][head]);
            float o0 = 0.f, o1 = 0.f, ls = 0.f;
#pragma unroll
            for (int j = 0; j < 8; ++j) {
                const int jw = jh * 8 + j;
                const float f = __expf(L.mw[jw][head] - M);
                const float2 ov = *(const float2*)((const float*)&L.kbuf[jw][0] + head * 128 + e0);
                o0 += f * ov.x; o1 += f * ov.y;
                ls += f * L.lw[jw][head];
            }
            o0 = x32_sum(o0); o1 = x32_sum(o1); ls = x32_sum(ls);
            const u32 p1 = (kvh * 2 + b) * 8 + s;
            const u32 sl = head * 64 + (w & 1) * 32 + lane;
            if (lane < 32) put2(G, O_1O + ((((kvh * 16 + (sl >> 4)) * 2 + b) * 8 + s) * 16 + (sl & 15)) * 16, tag, __float_as_uint(o0), __float_as_uint(o1));
            if (lane == 32 && (w & 1) == 0) put2(G, O_1ML + (p1 * 4 + head) * 16, tag, __float_as_uint(M), __float_as_uint(ls));
        }
        CSTAMP(18);
        if (!is_comm) return;
        STAMP(3);
        __builtin_amdgcn_s_sleep(64);
    } else if (!is_comm) {
        if (w >= 8) return;
        u32x4 wqr[4], wor[4], gur[4], wdr[2];
        __builtin_amdgcn_s_barrier();
        asm volatile("" ::: "memory");
#pragma unroll
        for (int t = 0; t < 4; ++t) wqr[t] = ldc(wq + (size_t)(w * 64 + t * 16 + li) * 4096 + kvh * 512 + ll * 32 + g * 8);
        asm volatile("" ::: "memory");
        u32x4 sel[2];
#pragma unroll
        for (int h = 0; h < 2; ++h) {
            const bool on = (g == 2 * h + (li >> 3));
            const u32 one = ((li & 1) ? 0x3F800000u : 0x00003F80u);
#pragma unroll
            for (int i = 0; i < 4; ++i) sel[h][i] = (on && ((li & 7) >> 1) == i) ? one : 0u;
        }
        barrier_raw();
        {
            f32x4 acc[2] = {zero4, zero4};
#pragma unroll
            for (int pp = 0; pp < 2; ++pp) {
                const u32x2 a0 = *(const u32x2*)&L.ln[li & 1][w * 64 + pp * 32 + g * 4];
                const u32x2 a1 = *(const u32x2*)&L.ln[li & 1][w * 64 + pp * 32 + 16 + g * 4];
                const u32x4 bf = {a0[0], a0[1], a1[0], a1[1]};
#pragma unroll
                for (int h = 0; h < 2; ++h)
                    acc[h] = mfma(pack2(mfma(wqr[2 * pp], sel[h], zero4), mfma(wqr[2 * pp + 1], sel[h], zero4)), bf, acc[h]);
            }
            if (li < 2) {
                *(f32x4*)&L.part[w][li * 32 + g * 4] = acc[0];
                *(f32x4*)&L.part[w][li * 32 + 16 + g * 4] = acc[1];
            }
        }
        barrier_raw();
        __builtin_amdgcn_s_barrier();
        asm volatile("" ::: "memory");
#pragma unroll
        for (int j = 0; j < 4; ++j)
            wor[j] = ldg(wo + (size_t)(kvh * 512 + ll * 32 + 8 * j + (lane >> 3)) * 512 + w * 64 + (lane & 7) * 8);
        {
            const u16* gu = is_mlp ? ((g < 2) ? wg : wu) + (size_t)(w * 64 + li) * FF + mc0 + (g & 1) * 8 : wo;
            const size_t gstep = is_mlp ? (size_t)16 * FF : 0;
#pragma unroll
            for (int t = 0; t < 4; ++t) gur[t] = ldc(gu + t * gstep);
            const u16* dp = is_mlp ? wd + (size_t)(mc0 + (lane >> 3)) * 512 + w * 64 + (lane & 7) * 8 : wo;
#pragma unroll
            for (int j = 0; j < 2; ++j) wdr[j] = ldg(dp + (is_mlp ? j * 8 * 512 : 0));
        }
        asm volatile("" ::: "memory");
        u32x4 guT[4];
#pragma unroll
        for (int pp = 0; pp < 2; ++pp)
#pragma unroll
            for (int mt = 0; mt < 2; ++mt)
                guT[pp * 2 + mt] = pack2(mfma(gur[2 * pp], sel[mt], zero4), mfma(gur[2 * pp + 1], sel[mt], zero4));
        asm volatile("" : "+v"(guT[0]), "+v"(guT[1]), "+v"(guT[2]), "+v"(guT[3]), "+v"(wor[0]), "+v"(wor[1]), "+v"(wor[2]), "+v"(wor[3]), "+v"(wdr[0]), "+v"(wdr[1]));
        barrier_raw();
        CSTAMP(19);
        const u32 tag = L.tag;
        const bool dvalid = (li >> 3) == (g >> 1);
        const int rep = (g >> 1) * 2 + (li >> 3);
        const u32 cp = w * 32 + ((g & 1) * 4 + rep) * 4 + ((li & 7) >> 1);
#pragma unroll
        for (int bb = 0; bb < 2; ++bb) {
            f32x4 d = zero4;
#pragma unroll
            for (int j = 0; j < 4; ++j) {
                const u32 pr = ((const u32*)L.os[bb])[4 * j + g];
                const u32 xv = (li >> 3) ? (pr >> 16) : (pr & 0xffffu);
                const u32 val = xv << sh;
                const u32x4 bf = {val & mk[0], val & mk[1], val & mk[2], val & mk[3]};
                d = mfma(wor[j], bf, d);
            }
            float y[4];
#pragma unroll
            for (int ii = 0; ii < 4; ++ii) {
                float v = dvalid ? d[ii] : 0.f;
                v += dppf<ROR8>(v);
                y[ii] = x32_sum(v);
            }
            const float v0 = (rep & 2) ? ((rep & 1) ? y[3] : y[2]) : ((rep & 1) ? y[1] : y[0]);
            const float v1 = dppf<XOR1>(v0);
            if ((li & 1) == 0)
                put2(G, O_2 + ((bb * 128 + (cp >> 1)) * 256 + (kvh * 16 + ll) * 2 + (cp & 1)) * 16, tag,
                     __float_as_uint(v0), __float_as_uint(v1));
        }
        CSTAMP(20);
        if (is_mlp) {
            barrier_raw();
            CSTAMP(21);
            f32x4 ag = zero4, au = zero4;
#pragma unroll
            for (int pp = 0; pp < 2; ++pp) {
                const u32x2 a0 = *(const u32x2*)&L.ln[li & 1][w * 64 + pp * 32 + g * 4];
                const u32x2 a1 = *(const u32x2*)&L.ln[li & 1][w * 64 + pp * 32 + 16 + g * 4];
                const u32x4 bf = {a0[0], a0[1], a1[0], a1[1]};
                ag = mfma(guT[pp * 2], bf, ag);
                au = mfma(guT[pp * 2 + 1], bf, au);
            }
            if (li < 2) {
                *(f32x4*)&L.part[w][li * 16 + g * 4] = ag;
                *(f32x4*)&L.part[w][32 + li * 16 + g * 4] = au;
            }
            CSTAMP(23);
            barrier_raw();
            barrier_raw();
            CSTAMP(24);
#pragma unroll
            for (int bb = 0; bb < 2; ++bb) {
                f32x4 d = zero4;
#pragma unroll
                for (int j = 0; j < 2; ++j) {
                    const u32 pr = ((const u32*)L.hh[bb])[4 * j + g];
                    const u32 xv = (li >> 3) ? (pr >> 16) : (pr & 0xffffu);
                    const u32 val = xv << sh;
                    const u32x4 bf = {val & mk[0], val & mk[1], val & mk[2], val & mk[3]};
                    d = mfma(wdr[j], bf, d);
                }
                float y[4];
#pragma unroll
                for (int ii = 0; ii < 4; ++ii) {
                    float v = dvalid ? d[ii] : 0.f;
                    v += dppf<ROR8>(v);
                    y[ii] = x32_sum(v);
                }
                const float v0 = (rep & 2) ? ((rep & 1) ? y[3] : y[2]) : ((rep & 1) ? y[1] : y[0]);
                const float v1 = dppf<XOR1>(v0);
                if ((li & 1) == 0)
                    put2(G, O_4 + ((bb * 128 + (cp >> 1)) * 256 + mblk * 2 + (cp & 1)) * 16, tag,
                         __float_as_uint(v0), __float_as_uint(v1));
            }
            CSTAMP(25);
        }
        return;
    }
    {
        const u32 tag = tagc;
        if (!is_attn) {
            u32x4 xr[2];
#pragma unroll
            for (int bb = 0; bb < 2; ++bb) xr[bb] = ldc(x + bb * 512 + lane * 8);
            asm volatile("" ::: "memory");
            __builtin_amdgcn_s_barrier();
            asm volatile("" ::: "memory");
            *(u32x4*)&L.ln[0][lane * 8] = ln_row(xr[0]);
            *(u32x4*)&L.ln[1][lane * 8] = ln_row(xr[1]);
            STAMP(1);
            barrier_raw();
            barrier_raw();
            {
                float q = 0.f;
#pragma unroll
                for (int j = 0; j < 8; ++j) q += L.part[j][lane];
                const u32 pr = pk(q, dppf<XOR1>(q));
                const u32 pr2 = dppu<XOR2>(pr);
                STAMP(26);
                if ((lane & 3) == 0) put2(G, O_Q + ((kvh * 2 + (lane >> 5)) * 128 + ll * 8 + ((lane & 31) >> 2)) * 16, tag, pr, pr2);
                asm volatile("" ::: "memory");
                __builtin_amdgcn_s_barrier();
                __builtin_amdgcn_s_sleep(100);
            }
            {
                const u32 head = ll >> 2;
                u32 off[5]; u32x4 v[5];
#pragma unroll
                for (int i = 0; i < 4; ++i)
                    off[i] = O_1O + ((kvh * 16 + ll) * 256 + i * 64 + lane) * 16;
                off[4] = O_1ML + (((kvh * 2 + (li >> 3)) * 8 + (li & 7)) * 4 + head) * 16;
                sweep<5>(G, off, tag, v);
                const float mj = __uint_as_float(v[4][0]), lj = __uint_as_float(v[4][2]);
                float M = fmaxf(mj, dppf<XOR1>(mj)); M = fmaxf(M, dppf<XOR2>(M)); M = fmaxf(M, dppf<HMIR>(M));
                const float wj = __expf(mj - M);
                float ls = wj * lj;
                ls += dppf<XOR1>(ls); ls += dppf<XOR2>(ls); ls += dppf<HMIR>(ls);
                const float wn = wj * __builtin_amdgcn_rcpf(ls);
                float o[2][2] = {{0.f, 0.f}, {0.f, 0.f}};
#pragma unroll
                for (int i = 0; i < 4; ++i) {
                    const int base = (i >> 1) * 8 + (i & 1) * 4;
                    const float w0 = __builtin_bit_cast(float, __builtin_amdgcn_readlane(__builtin_bit_cast(int, wn), base));
                    const float w1 = __builtin_bit_cast(float, __builtin_amdgcn_readlane(__builtin_bit_cast(int, wn), base + 1));
                    const float w2 = __builtin_bit_cast(float, __builtin_amdgcn_readlane(__builtin_bit_cast(int, wn), base + 2));
                    const float w3 = __builtin_bit_cast(float, __builtin_amdgcn_readlane(__builtin_bit_cast(int, wn), base + 3));
                    const float ww = (g & 2) ? ((g & 1) ? w3 : w2) : ((g & 1) ? w1 : w0);
                    o[i >> 1][0] += ww * __uint_as_float(v[i][0]);
                    o[i >> 1][1] += ww * __uint_as_float(v[i][2]);
                }
#pragma unroll
                for (int bb = 0; bb < 2; ++bb) {
                    const float a0 = xrow_sum(o[bb][0]), a1 = xrow_sum(o[bb][1]);
                    if (lane < 16) ((u32*)L.os[bb])[lane] = pk(a0, a1);
                }
            }
            STAMP(4);
            barrier_raw();
        }
        float h0, h1;
        {
            u32 off[4]; u32x4 v[4];
#pragma unroll
            for (int i = 0; i < 4; ++i) off[i] = O_2 + (bid * 256 + i * 64 + lane) * 16;
            sweep<4>(G, off, tag, v);
            float s0 = 0.f, s1 = 0.f;
#pragma unroll
            for (int i = 0; i < 4; ++i) { s0 += __uint_as_float(v[i][0]); s1 += __uint_as_float(v[i][2]); }
            s0 += dppf<XOR2>(s0); s0 += dppf<ROR4>(s0); s0 += dppf<ROR8>(s0); s0 = xrow_sum(s0);
            s1 += dppf<XOR2>(s1); s1 += dppf<ROR4>(s1); s1 += dppf<ROR8>(s1); s1 = xrow_sum(s1);
            const u32 a = pk(s0, s1);
            const u32 pr = pk(lo2f(xpair) + lo2f(a), hi2f(xpair) + hi2f(a));
            h0 = lo2f(pr); h1 = hi2f(pr);
            const u32 pr2 = dppu<XOR1>(pr);
            if (lane == 0) put2(G, O_3 + bid * 16, tag, pr, pr2);
        }
        STAMP(5);
        if (is_mlp) {
            u32 off[4]; u32x4 v[4];
#pragma unroll
            for (int i = 0; i < 4; ++i) off[i] = O_3 + (i * 64 + lane) * 16;
            sweep<4>(G, off, tag, v);
            STAMP(6);
#pragma unroll
            for (int bb = 0; bb < 2; ++bb) {
                const u32x4 hr = {v[2 * bb][0], v[2 * bb][2], v[2 * bb + 1][0], v[2 * bb + 1][2]};
                const u32x4 ln = ln_row(hr);
                *(u32x2*)&L.ln[bb][lane * 4] = u32x2{ln[0], ln[1]};
                *(u32x2*)&L.ln[bb][256 + lane * 4] = u32x2{ln[2], ln[3]};
            }
            barrier_raw();
            barrier_raw();
            STAMP(7);
            {
                float a = 0.f;
#pragma unroll
                for (int j = 0; j < 8; ++j) a += L.part[j][lane];
                a = bfr(a);
                auto t = __builtin_amdgcn_permlane32_swap(__float_as_uint(a), __float_as_uint(a), false, false);
                const float u = __uint_as_float(t[1]);
                const float sg = bfr(__builtin_amdgcn_rcpf(1.0f + __expf(-a)));
                const float hv = bfr(sg * u);
                if (lane < 32) L.hh[lane >> 4][lane & 15] = (u16)(__float_as_uint(hv) >> 16);
            }
            barrier_raw();
        }
        else __builtin_amdgcn_s_sleep(90);
        STAMP(8);
        {
            u32 off[4]; u32x4 v[4];
#pragma unroll
            for (int i = 0; i < 4; ++i) {
                const u32 pidx = i * 32 + (lane >> 1);
                off[i] = O_4 + (bid * 256 + (pidx < NMLP ? pidx : pidx - 16) * 2 + (lane & 1)) * 16;
            }
            sweep<4>(G, off, tag, v);
            float s0 = 0.f, s1 = 0.f;
#pragma unroll
            for (int i = 0; i < 4; ++i) {
                const bool live = (i * 32 + (lane >> 1)) < NMLP;
                s0 += live ? __uint_as_float(v[i][0]) : 0.f;
                s1 += live ? __uint_as_float(v[i][2]) : 0.f;
            }
            s0 += dppf<XOR2>(s0); s0 += dppf<ROR4>(s0); s0 += dppf<ROR8>(s0); s0 = xrow_sum(s0);
            s1 += dppf<XOR2>(s1); s1 += dppf<ROR4>(s1); s1 += dppf<ROR8>(s1); s1 = xrow_sum(s1);
            const u32 a = pk(s0, s1);
            const u32 res = pk(h0 + lo2f(a), h1 + hi2f(a));
            if (lane < 2) *(u32*)(out + rb * 512 + rc) = res;
        }
        STAMP(9);
        if (bid == 0 && lane == 0) __hip_atomic_store((gu32*)(wsg), tag, RLX_AGENT);
    }
}

extern "C" void kernel_launch(void* const* d_in, const int* in_sizes, int n_in,
                              void* d_out, int out_size, void* d_ws, size_t ws_size,
                              hipStream_t stream) {
    mega<<<NBLK, NTHR, 0, stream>>>((const u16*)d_in[0], (const u16*)d_in[1], (const u16*)d_in[2], (const u16*)d_in[3],
                                    (const u16*)d_in[4], (const u16*)d_in[6], (const u16*)d_in[7], (const u16*)d_in[8],
                                    (u16*)d_out, (u32*)d_ws);
}
```

```cpp
#include <hip/hip_runtime.h>
#include <stdint.h>

typedef unsigned short u16;
typedef unsigned int u32;
typedef unsigned long long u64;
typedef __attribute__((ext_vector_type(4))) unsigned u32x4;
typedef __attribute__((ext_vector_type(2))) unsigned u32x2;
typedef __attribute__((ext_vector_type(8))) short s16x8;
typedef __attribute__((ext_vector_type(4))) float f32x4;
typedef __attribute__((ext_vector_type(2))) __bf16 bf16x2;
#define GAS __attribute__((address_space(1)))
#define LAS __attribute__((address_space(3)))
typedef GAS u32 gu32;
#define RLX_AGENT __ATOMIC_RELAXED, __HIP_MEMORY_SCOPE_AGENT

constexpr int SK = 32768, WIN = 4096, FF = 1792;
constexpr int NBLK = 256, NTHR = 1024, NMLP = 112;
constexpr unsigned SPIN_CAP = 1u << 18;

constexpr u32 O_Q = 256;
constexpr u32 O_1O = O_Q + 8 * 2 * 128 * 16;
constexpr u32 O_1ML = O_1O + 128 * 256 * 16;
constexpr u32 O_2 = O_1ML + 128 * 4 * 16;
constexpr u32 O_3 = O_2 + 128 * 512 * 16;
constexpr u32 O_4 = O_3 + 256 * 16;
constexpr u32 O_END = O_4 + 256 * 256 * 16;


__device__ __forceinline__ float lo2f(u32 v) { return __uint_as_float(v << 16); }
__device__ __forceinline__ float hi2f(u32 v) { return __uint_as_float(v & 0xffff0000u); }
__device__ __forceinline__ u32 pk(float lo, float hi) {
    const bf16x2 v = {(__bf16)lo, (__bf16)hi};
    return __builtin_bit_cast(u32, v);
}
__device__ __forceinline__ float bfr(float f) { return lo2f(pk(f, f)); }
__device__ __forceinline__ u32 packhi(float lo, float hi) {
    return __builtin_amdgcn_perm(__float_as_uint(hi), __float_as_uint(lo), 0x07060302u);
}
__device__ __forceinline__ u32x4 ldg(const u16* p) { return __builtin_nontemporal_load((const u32x4*)p); }
__device__ __forceinline__ u32x4 ldc(const u16* p) { return *(const u32x4*)p; }
__device__ __forceinline__ f32x4 mfma(u32x4 a, u32x4 b, f32x4 c) {
    return __builtin_amdgcn_mfma_f32_16x16x32_bf16(__builtin_bit_cast(s16x8, a), __builtin_bit_cast(s16x8, b), c, 0, 0, 0);
}
__device__ __forceinline__ u32x4 pack2(f32x4 t0, f32x4 t1) {
    u32x4 r;
    r[0] = packhi(t0[0], t0[1]); r[1] = packhi(t0[2], t0[3]);
    r[2] = packhi(t1[0], t1[1]); r[3] = packhi(t1[2], t1[3]);
    return r;
}
template <int CTRL> __device__ __forceinline__ float dppf(float x) {
    return __builtin_bit_cast(float, __builtin_amdgcn_update_dpp(0, __builtin_bit_cast(int, x), CTRL, 0xf, 0xf, true));
}
template <int CTRL> __device__ __forceinline__ u32 dppu(u32 x) {
    return (u32)__builtin_amdgcn_update_dpp(0, (int)x, CTRL, 0xf, 0xf, true);
}
constexpr int XOR1 = 0xB1, XOR2 = 0x4E, HMIR = 0x141, MIR = 0x140, ROR4 = 0x124, ROR8 = 0x128;
__device__ __forceinline__ float x32_sum(float x) {
    auto t = __builtin_amdgcn_permlane32_swap(__float_as_uint(x), __float_as_uint(x), false, false);
    return __uint_as_float(t[0]) + __uint_as_float(t[1]);
}
__device__ __forceinline__ float xrow_sum(float x) {
    auto s = __builtin_amdgcn_permlane16_swap(__float_as_uint(x), __float_as_uint(x), false, false);
    return x32_sum(__uint_as_float(s[0]) + __uint_as_float(s[1]));
}
__device__ __forceinline__ float xrow_max(float x) {
    auto s = __builtin_amdgcn_permlane16_swap(__float_as_uint(x), __float_as_uint(x), false, false);
    x = fmaxf(__uint_as_float(s[0]), __uint_as_float(s[1]));
    auto t = __builtin_amdgcn_permlane32_swap(__float_as_uint(x), __float_as_uint(x), false, false);
    return fmaxf(__uint_as_float(t[0]), __uint_as_float(t[1]));
}
__device__ __forceinline__ float wsum(float v) {
    v += dppf<XOR1>(v); v += dppf<XOR2>(v); v += dppf<HMIR>(v); v += dppf<MIR>(v);
    return xrow_sum(v);
}
__device__ __forceinline__ void barrier_raw() {
    asm volatile("s_waitcnt lgkmcnt(0)" ::: "memory");
    __builtin_amdgcn_s_barrier();
    asm volatile("" ::: "memory");
}
typedef __amdgpu_buffer_rsrc_t rsrc_t;
__device__ __forceinline__ void glds16(const u16* gsrc, u32 lds_dst) {
    u32 keep;
    asm volatile("s_mov_b32 %0, m0\n\ts_mov_b32 m0, %2\n\ts_nop 0\n\tglobal_load_lds_dwordx4 %1, off nt\n\ts_mov_b32 m0, %0"
                 : "=&s"(keep) : "v"(gsrc), "s"(lds_dst) : "memory");
}
__device__ __forceinline__ void put2(rsrc_t rs, u32 off, u32 tag, u32 v0, u32 v1) {
    const u32x4 d = {v0, tag, v1, tag};
    __builtin_amdgcn_raw_buffer_store_b128(d, rs, (int)off, 0, 16  );
}
template <int N>
__device__ __forceinline__ void sweep(rsrc_t rs, const u32 (&off)[N], u32 tag, u32x4 (&v)[N]) {
    for (unsigned spins = 0; spins < SPIN_CAP; ++spins) {
        asm volatile("" ::: "memory");
        bool ok = true;
#pragma unroll
        for (int k = 0; k < N; ++k) v[k] = __builtin_amdgcn_raw_buffer_load_b128(rs, (int)off[k], 0, 16  );
#pragma unroll
        for (int k = 0; k < N; ++k) ok &= (v[k][1] == tag) & (v[k][3] == tag);
        if (__all(ok)) return;
    }
}

__device__ __forceinline__ u32x4 ln_row(u32x4 xr) {
    float v[8];
#pragma unroll
    for (int i = 0; i < 4; ++i) { v[2 * i] = lo2f(xr[i]); v[2 * i + 1] = hi2f(xr[i]); }
    const float s = ((v[0] + v[1]) + (v[2] + v[3])) + ((v[4] + v[5]) + (v[6] + v[7]));
    const float mean = wsum(s) * (1.0f / 512.0f);
    float s2 = 0.f;
#pragma unroll
    for (int i = 0; i < 8; ++i) { const float d = v[i] - mean; s2 += d * d; }
    const float var = wsum(s2) * (1.0f / 512.0f);
    const float mb = bfr(mean);
    const float den = bfr(__builtin_amdgcn_sqrtf(bfr(bfr(var) + 1e-5f)));
    const float inv = __builtin_amdgcn_rcpf(den);
    u32x4 out;
#pragma unroll
    for (int i = 0; i < 4; ++i) {
        const u32 d = pk(v[2 * i] - mb, v[2 * i + 1] - mb);
        out[i] = pk(lo2f(d) * inv, hi2f(d) * inv);
    }
    return out;
}

struct __attribute__((aligned(16))) Lds {
    u16 kbuf[16][32 * 128];
    u16 pl[16][4][4][8];
    float mw[16][4], lw[16][4];
    float part[8][64];
    u16 ln[2][512];
    u16 qs[512];
    u16 os[2][32];
    u16 hh[2][16];
    u32 tag;
};

__global__ void __launch_bounds__(NTHR) mega(const u16* x, const u16* kc, const u16* vc, const u16* wq, const u16* wo,
                                             const u16* wg, const u16* wu, const u16* wd, u16* out, u32* wsg) {
    __shared__ Lds L;
    const int tid = threadIdx.x, lane = tid & 63;
    const int w = __builtin_amdgcn_readfirstlane(tid >> 6);
    const int bid = blockIdx.x;
    const int kvh = bid & 7, idx = bid >> 3;
    const bool is_attn = idx < 16;
    const int b = (idx >> 3) & 1, s = idx & 7;
    const int ll = idx - 16;
    const bool is_mlp = ll >= 0 && ll < 14;
    const int mblk = kvh * 14 + ll, mc0 = mblk * 16;
    const rsrc_t G = __builtin_amdgcn_make_buffer_rsrc((void*)wsg, 0, (int)O_END, 0x00020000);
    const int g = lane >> 4, li = lane & 15;
    const f32x4 zero4 = {0.f, 0.f, 0.f, 0.f};
    u32 mk[4];
#pragma unroll
    for (int d = 0; d < 4; ++d) mk[d] = (((li & 7) >> 1) == d) ? 0xFFFFFFFFu : 0u;
    const u32 sh = 16 * (li & 1);

    const bool is_comm = (w == 15);
    const int rb = bid >> 7, rc = (bid & 127) * 4 + (lane & 1) * 2;
    u32 tagc = 0, xpair = 0;
    if (is_comm) {
        tagc = __hip_atomic_load((gu32*)(wsg), RLX_AGENT) + 1u;
        xpair = *(const u32*)(x + rb * 512 + rc);
        if (lane == 0) L.tag = tagc;
    }
    if (is_attn) {
        u32x4 vr[8];
        const size_t kvoff = ((size_t)(b * 8 + kvh) * SK + (SK - WIN) + s * 512 + w * 32) * 128;
        {
            const u32 kdst = (u32)(uintptr_t)(LAS u16*)&L.kbuf[w][0];
#pragma unroll
            for (int j = 0; j < 8; ++j)
                glds16(kc + kvoff + (size_t)(4 * j + g) * 128 + ((li ^ (4 * (j & 3) + g)) * 8), kdst + j * 1024);
        }
        if (is_comm) {
            u32 off[2]; u32x4 v[2];
#pragma unroll
            for (int k = 0; k < 2; ++k) off[k] = O_Q + ((kvh * 2 + b) * 128 + k * 64 + lane) * 16;
            sweep<2>(G, off, tagc, v);
#pragma unroll
            for (int k = 0; k < 2; ++k) *(u32x2*)&L.qs[(k * 64 + lane) * 4] = u32x2{v[k][0], v[k][2]};
            barrier_raw();
        }
        if (!is_comm) barrier_raw();
#pragma unroll
        for (int j = 0; j < 8; ++j) vr[j] = ldg(vc + kvoff + (size_t)j * 512 + lane * 8);
        asm volatile("" ::: "memory");
        float* ow = (float*)&L.kbuf[w][0];
        {
            asm volatile("s_waitcnt vmcnt(8)" ::: "memory");
            f32x4 st[2] = {zero4, zero4};
#pragma unroll
            for (int kk = 0; kk < 4; ++kk) {
                const u32x4 qf = *(const u32x4*)&L.qs[(li & 3) * 128 + kk * 32 + g * 8];
#pragma unroll
                for (int t = 0; t < 2; ++t) {
                    const u32x4 kf = *(const u32x4*)&L.kbuf[w][(t * 16 + li) * 128 + (((kk * 4 + g) ^ li) * 8)];
                    st[t] = mfma(kf, qf, st[t]);
                }
            }
            float sc[8];
#pragma unroll
            for (int t = 0; t < 2; ++t)
#pragma unroll
                for (int i = 0; i < 2; ++i) {
                    const u32 a = pk(st[t][2 * i], st[t][2 * i + 1]);
                    const u32 c = pk(lo2f(a) * 0.08838834764831845f, hi2f(a) * 0.08838834764831845f);
                    sc[t * 4 + 2 * i] = lo2f(c); sc[t * 4 + 2 * i + 1] = hi2f(c);
                }
            float m = fmaxf(fmaxf(fmaxf(sc[0], sc[1]), fmaxf(sc[2], sc[3])), fmaxf(fmaxf(sc[4], sc[5]), fmaxf(sc[6], sc[7])));
            m = xrow_max(m);
            float p[8], l = 0.f;
#pragma unroll
            for (int i = 0; i < 8; ++i) { p[i] = __expf(sc[i] - m); l += p[i]; }
            l = xrow_sum(l);
#pragma unroll
            for (int t = 0; t < 2; ++t)
#pragma unroll
                for (int i = 0; i < 2; ++i) {
                    const u32 pp = pk(p[t * 4 + 2 * i], p[t * 4 + 2 * i + 1]);
                    L.pl[w][li & 3][2 * i][t * 4 + g] = (u16)(pp & 0xffffu);
                    L.pl[w][li & 3][2 * i + 1][t * 4 + g] = (u16)(pp >> 16);
                }
            if (lane < 4) { L.mw[w][lane] = m; L.lw[w][lane] = l; }
            asm volatile("s_waitcnt lgkmcnt(0)" ::: "memory");
            u32x4 pv[2];
#pragma unroll
            for (int hp = 0; hp < 2; ++hp) pv[hp] = *(const u32x4*)&L.pl[w][2 * hp + (li >> 3)][g][0];
            f32x4 oacc[2] = {zero4, zero4};
#pragma unroll
            for (int j = 0; j < 8; ++j)
#pragma unroll
                for (int hp = 0; hp < 2; ++hp) {
                    const u32 xv = (j & 1) ? (pv[hp][j >> 1] >> 16) : (pv[hp][j >> 1] & 0xffffu);
                    const u32 val = xv << sh;
                    const u32x4 bf = {val & mk[0], val & mk[1], val & mk[2], val & mk[3]};
                    oacc[hp] = mfma(vr[j], bf, oacc[hp]);
                }
#pragma unroll
            for (int hp = 0; hp < 2; ++hp)
#pragma unroll
                for (int ii = 0; ii < 4; ++ii) ow[(2 * hp + (li >> 3)) * 128 + (g * 4 + ii) * 8 + (li & 7)] = oacc[hp][ii];
        }
        barrier_raw();
        if (w < 8) {
            const u32 tag = L.tag;
            const int head = w >> 1, e0 = (w & 1) * 64 + (lane & 31) * 2, jh = lane >> 5;
            float M = L.mw[0][head];
#pragma unroll
            for (int j = 1; j < 16; ++j) M = fmaxf(M, L.mw[j][head]);
            float o0 = 0.f, o1 = 0.f, ls = 0.f;
#pragma unroll
            for (int j = 0; j < 8; ++j) {
                const int jw = jh * 8 + j;
                const float f = __expf(L.mw[jw][head] - M);
                const float2 ov = *(const float2*)((const float*)&L.kbuf[jw][0] + head * 128 + e0);
                o0 += f * ov.x; o1 += f * ov.y;
                ls += f * L.lw[jw][head];
            }
            o0 = x32_sum(o0); o1 = x32_sum(o1); ls = x32_sum(ls);
            const u32 p1 = (kvh * 2 + b) * 8 + s;
            const u32 sl = head * 64 + (w & 1) * 32 + lane;
            if (lane < 32) put2(G, O_1O + ((((kvh * 16 + (sl >> 4)) * 2 + b) * 8 + s) * 16 + (sl & 15)) * 16, tag, __float_as_uint(o0), __float_as_uint(o1));
            if (lane == 32 && (w & 1) == 0) put2(G, O_1ML + (p1 * 4 + head) * 16, tag, __float_as_uint(M), __float_as_uint(ls));
        }
        if (!is_comm) return;
        __builtin_amdgcn_s_sleep(64);
    } else if (!is_comm) {
        if (w >= 8) return;
        u32x4 wqr[4], wor[4], gur[4], wdr[2];
        __builtin_amdgcn_s_barrier();
        asm volatile("" ::: "memory");
#pragma unroll
        for (int t = 0; t < 4; ++t) wqr[t] = ldc(wq + (size_t)(w * 64 + t * 16 + li) * 4096 + kvh * 512 + ll * 32 + g * 8);
        asm volatile("" ::: "memory");
        u32x4 sel[2];
#pragma unroll
        for (int h = 0; h < 2; ++h) {
            const bool on = (g == 2 * h + (li >> 3));
            const u32 one = ((li & 1) ? 0x3F800000u : 0x00003F80u);
#pragma unroll
            for (int i = 0; i < 4; ++i) sel[h][i] = (on && ((li & 7) >> 1) == i) ? one : 0u;
        }
        barrier_raw();
        {
            f32x4 acc[2] = {zero4, zero4};
#pragma unroll
            for (int pp = 0; pp < 2; ++pp) {
                const u32x2 a0 = *(const u32x2*)&L.ln[li & 1][w * 64 + pp * 32 + g * 4];
                const u32x2 a1 = *(const u32x2*)&L.ln[li & 1][w * 64 + pp * 32 + 16 + g * 4];
                const u32x4 bf = {a0[0], a0[1], a1[0], a1[1]};
#pragma unroll
                for (int h = 0; h < 2; ++h)
                    acc[h] = mfma(pack2(mfma(wqr[2 * pp], sel[h], zero4), mfma(wqr[2 * pp + 1], sel[h], zero4)), bf, acc[h]);
            }
            if (li < 2) {
                *(f32x4*)&L.part[w][li * 32 + g * 4] = acc[0];
                *(f32x4*)&L.part[w][li * 32 + 16 + g * 4] = acc[1];
            }
        }
        barrier_raw();
        __builtin_amdgcn_s_barrier();
        asm volatile("" ::: "memory");
#pragma unroll
        for (int j = 0; j < 4; ++j)
            wor[j] = ldg(wo + (size_t)(kvh * 512 + ll * 32 + 8 * j + (lane >> 3)) * 512 + w * 64 + (lane & 7) * 8);
        {
            const u16* gu = is_mlp ? ((g < 2) ? wg : wu) + (size_t)(w * 64 + li) * FF + mc0 + (g & 1) * 8 : wo;
            const size_t gstep = is_mlp ? (size_t)16 * FF : 0;
#pragma unroll
            for (int t = 0; t < 4; ++t) gur[t] = ldc(gu + t * gstep);
            const u16* dp = is_mlp ? wd + (size_t)(mc0 + (lane >> 3)) * 512 + w * 64 + (lane & 7) * 8 : wo;
#pragma unroll
            for (int j = 0; j < 2; ++j) wdr[j] = ldg(dp + (is_mlp ? j * 8 * 512 : 0));
        }
        asm volatile("" ::: "memory");
        u32x4 guT[4];
#pragma unroll
        for (int pp = 0; pp < 2; ++pp)
#pragma unroll
            for (int mt = 0; mt < 2; ++mt)
                guT[pp * 2 + mt] = pack2(mfma(gur[2 * pp], sel[mt], zero4), mfma(gur[2 * pp + 1], sel[mt], zero4));
        asm volatile("" : "+v"(guT[0]), "+v"(guT[1]), "+v"(guT[2]), "+v"(guT[3]), "+v"(wor[0]), "+v"(wor[1]), "+v"(wor[2]), "+v"(wor[3]), "+v"(wdr[0]), "+v"(wdr[1]));
        barrier_raw();
        const u32 tag = L.tag;
        const bool dvalid = (li >> 3) == (g >> 1);
        const int rep = (g >> 1) * 2 + (li >> 3);
        const u32 cp = w * 32 + ((g & 1) * 4 + rep) * 4 + ((li & 7) >> 1);
#pragma unroll
        for (int bb = 0; bb < 2; ++bb) {
            f32x4 d = zero4;
#pragma unroll
            for (int j = 0; j < 4; ++j) {
                const u32 pr = ((const u32*)L.os[bb])[4 * j + g];
                const u32 xv = (li >> 3) ? (pr >> 16) : (pr & 0xffffu);
                const u32 val = xv << sh;
                const u32x4 bf = {val & mk[0], val & mk[1], val & mk[2], val & mk[3]};
                d = mfma(wor[j], bf, d);
            }
            float y[4];
#pragma unroll
            for (int ii = 0; ii < 4; ++ii) {
                float v = dvalid ? d[ii] : 0.f;
                v += dppf<ROR8>(v);
                y[ii] = x32_sum(v);
            }
            const float v0 = (rep & 2) ? ((rep & 1) ? y[3] : y[2]) : ((rep & 1) ? y[1] : y[0]);
            const float v1 = dppf<XOR1>(v0);
            if ((li & 1) == 0)
                put2(G, O_2 + ((bb * 128 + (cp >> 1)) * 256 + (kvh * 16 + ll) * 2 + (cp & 1)) * 16, tag,
                     __float_as_uint(v0), __float_as_uint(v1));
        }
        if (is_mlp) {
            barrier_raw();
            f32x4 ag = zero4, au = zero4;
#pragma unroll
            for (int pp = 0; pp < 2; ++pp) {
                const u32x2 a0 = *(const u32x2*)&L.ln[li & 1][w * 64 + pp * 32 + g * 4];
                const u32x2 a1 = *(const u32x2*)&L.ln[li & 1][w * 64 + pp * 32 + 16 + g * 4];
                const u32x4 bf = {a0[0], a0[1], a1[0], a1[1]};
                ag = mfma(guT[pp * 2], bf, ag);
                au = mfma(guT[pp * 2 + 1], bf, au);
            }
            if (li < 2) {
                *(f32x4*)&L.part[w][li * 16 + g * 4] = ag;
                *(f32x4*)&L.part[w][32 + li * 16 + g * 4] = au;
            }
            barrier_raw();
            barrier_raw();
#pragma unroll
            for (int bb = 0; bb < 2; ++bb) {
                f32x4 d = zero4;
#pragma unroll
                for (int j = 0; j < 2; ++j) {
                    const u32 pr = ((const u32*)L.hh[bb])[4 * j + g];
                    const u32 xv = (li >> 3) ? (pr >> 16) : (pr & 0xffffu);
                    const u32 val = xv << sh;
                    const u32x4 bf = {val & mk[0], val & mk[1], val & mk[2], val & mk[3]};
                    d = mfma(wdr[j], bf, d);
                }
                float y[4];
#pragma unroll
                for (int ii = 0; ii < 4; ++ii) {
                    float v = dvalid ? d[ii] : 0.f;
                    v += dppf<ROR8>(v);
                    y[ii] = x32_sum(v);
                }
                const float v0 = (rep & 2) ? ((rep & 1) ? y[3] : y[2]) : ((rep & 1) ? y[1] : y[0]);
                const float v1 = dppf<XOR1>(v0);
                if ((li & 1) == 0)
                    put2(G, O_4 + ((bb * 128 + (cp >> 1)) * 256 + mblk * 2 + (cp & 1)) * 16, tag,
                         __float_as_uint(v0), __float_as_uint(v1));
            }
        }
        return;
    }
    {
        const u32 tag = tagc;
        if (!is_attn) {
            u32x4 xr[2];
#pragma unroll
            for (int bb = 0; bb < 2; ++bb) xr[bb] = ldc(x + bb * 512 + lane * 8);
            asm volatile("" ::: "memory");
            __builtin_amdgcn_s_barrier();
            asm volatile("" ::: "memory");
            *(u32x4*)&L.ln[0][lane * 8] = ln_row(xr[0]);
            *(u32x4*)&L.ln[1][lane * 8] = ln_row(xr[1]);
            barrier_raw();
            barrier_raw();
            {
                float q = 0.f;
#pragma unroll
                for (int j = 0; j < 8; ++j) q += L.part[j][lane];
                const u32 pr = pk(q, dppf<XOR1>(q));
                const u32 pr2 = dppu<XOR2>(pr);
                if ((lane & 3) == 0) put2(G, O_Q + ((kvh * 2 + (lane >> 5)) * 128 + ll * 8 + ((lane & 31) >> 2)) * 16, tag, pr, pr2);
                asm volatile("" ::: "memory");
                __builtin_amdgcn_s_barrier();
                __builtin_amdgcn_s_sleep(100);
            }
            {
                const u32 head = ll >> 2;
                u32 off[5]; u32x4 v[5];
#pragma unroll
                for (int i = 0; i < 4; ++i)
                    off[i] = O_1O + ((kvh * 16 + ll) * 256 + i * 64 + lane) * 16;
                off[4] = O_1ML + (((kvh * 2 + (li >> 3)) * 8 + (li & 7)) * 4 + head) * 16;
                sweep<5>(G, off, tag, v);
                const float mj = __uint_as_float(v[4][0]), lj = __uint_as_float(v[4][2]);
                float M = fmaxf(mj, dppf<XOR1>(mj)); M = fmaxf(M, dppf<XOR2>(M)); M = fmaxf(M, dppf<HMIR>(M));
                const float wj = __expf(mj - M);
                float ls = wj * lj;
                ls += dppf<XOR1>(ls); ls += dppf<XOR2>(ls); ls += dppf<HMIR>(ls);
                const float wn = wj * __builtin_amdgcn_rcpf(ls);
                float o[2][2] = {{0.f, 0.f}, {0.f, 0.f}};
#pragma unroll
                for (int i = 0; i < 4; ++i) {
                    const int base = (i >> 1) * 8 + (i & 1) * 4;
                    const float w0 = __builtin_bit_cast(float, __builtin_amdgcn_readlane(__builtin_bit_cast(int, wn), base));
                    const float w1 = __builtin_bit_cast(float, __builtin_amdgcn_readlane(__builtin_bit_cast(int, wn), base + 1));
                    const float w2 = __builtin_bit_cast(float, __builtin_amdgcn_readlane(__builtin_bit_cast(int, wn), base + 2));
                    const float w3 = __builtin_bit_cast(float, __builtin_amdgcn_readlane(__builtin_bit_cast(int, wn), base + 3));
                    const float ww = (g & 2) ? ((g & 1) ? w3 : w2) : ((g & 1) ? w1 : w0);
                    o[i >> 1][0] += ww * __uint_as_float(v[i][0]);
                    o[i >> 1][1] += ww * __uint_as_float(v[i][2]);
                }
#pragma unroll
                for (int bb = 0; bb < 2; ++bb) {
                    const float a0 = xrow_sum(o[bb][0]), a1 = xrow_sum(o[bb][1]);
                    if (lane < 16) ((u32*)L.os[bb])[lane] = pk(a0, a1);
                }
            }
            barrier_raw();
        }
        float h0, h1;
        {
            u32 off[4]; u32x4 v[4];
#pragma unroll
            for (int i = 0; i < 4; ++i) off[i] = O_2 + (bid * 256 + i * 64 + lane) * 16;
            sweep<4>(G, off, tag, v);
            float s0 = 0.f, s1 = 0.f;
#pragma unroll
            for (int i = 0; i < 4; ++i) { s0 += __uint_as_float(v[i][0]); s1 += __uint_as_float(v[i][2]); }
            s0 += dppf<XOR2>(s0); s0 += dppf<ROR4>(s0); s0 += dppf<ROR8>(s0); s0 = xrow_sum(s0);
            s1 += dppf<XOR2>(s1); s1 += dppf<ROR4>(s1); s1 += dppf<ROR8>(s1); s1 = xrow_sum(s1);
            const u32 a = pk(s0, s1);
            const u32 pr = pk(lo2f(xpair) + lo2f(a), hi2f(xpair) + hi2f(a));
            h0 = lo2f(pr); h1 = hi2f(pr);
            const u32 pr2 = dppu<XOR1>(pr);
            if (lane == 0) put2(G, O_3 + bid * 16, tag, pr, pr2);
        }
        if (is_mlp) {
            u32 off[4]; u32x4 v[4];
#pragma unroll
            for (int i = 0; i < 4; ++i) off[i] = O_3 + (i * 64 + lane) * 16;
            sweep<4>(G, off, tag, v);
#pragma unroll
            for (int bb = 0; bb < 2; ++bb) {
                const u32x4 hr = {v[2 * bb][0], v[2 * bb][2], v[2 * bb + 1][0], v[2 * bb + 1][2]};
                const u32x4 ln = ln_row(hr);
                *(u32x2*)&L.ln[bb][lane * 4] = u32x2{ln[0], ln[1]};
                *(u32x2*)&L.ln[bb][256 + lane * 4] = u32x2{ln[2], ln[3]};
            }
            barrier_raw();
            barrier_raw();
            {
                float a = 0.f;
#pragma unroll
                for (int j = 0; j < 8; ++j) a += L.part[j][lane];
                a = bfr(a);
                auto t = __builtin_amdgcn_permlane32_swap(__float_as_uint(a), __float_as_uint(a), false, false);
                const float u = __uint_as_float(t[1]);
                const float sg = bfr(__builtin_amdgcn_rcpf(1.0f + __expf(-a)));
                const float hv = bfr(sg * u);
                if (lane < 32) L.hh[lane >> 4][lane & 15] = (u16)(__float_as_uint(hv) >> 16);
            }
            barrier_raw();
        }
        else __builtin_amdgcn_s_sleep(90);
        {
            u32 off[4]; u32x4 v[4];
#pragma unroll
            for (int i = 0; i < 4; ++i) {
                const u32 pidx = i * 32 + (lane >> 1);
                off[i] = O_4 + (bid * 256 + (pidx < NMLP ? pidx : pidx - 16) * 2 + (lane & 1)) * 16;
            }
            sweep<4>(G, off, tag, v);
            float s0 = 0.f, s1 = 0.f;
#pragma unroll
            for (int i = 0; i < 4; ++i) {
                const bool live = (i * 32 + (lane >> 1)) < NMLP;
                s0 += live ? __uint_as_float(v[i][0]) : 0.f;
                s1 += live ? __uint_as_float(v[i][2]) : 0.f;
            }
            s0 += dppf<XOR2>(s0); s0 += dppf<ROR4>(s0); s0 += dppf<ROR8>(s0); s0 = xrow_sum(s0);
            s1 += dppf<XOR2>(s1); s1 += dppf<ROR4>(s1); s1 += dppf<ROR8>(s1); s1 = xrow_sum(s1);
            const u32 a = pk(s0, s1);
            const u32 res = pk(h0 + lo2f(a), h1 + hi2f(a));
            if (lane < 2) *(u32*)(out + rb * 512 + rc) = res;
        }
        if (bid == 0 && lane == 0) __hip_atomic_store((gu32*)(wsg), tag, RLX_AGENT);
    }
}

extern "C" void kernel_launch(void* const* d_in, const int* in_sizes, int n_in,
                              void* d_out, int out_size, void* d_ws, size_t ws_size,
                              hipStream_t stream) {
    mega<<<NBLK, NTHR, 0, stream>>>((const u16*)d_in[0], (const u16*)d_in[1], (const u16*)d_in[2], (const u16*)d_in[3],
                                    (const u16*)d_in[4], (const u16*)d_in[6], (const u16*)d_in[7], (const u16*)d_in[8],
                                    (u16*)d_out, (u32*)d_ws);
}
```
